```python
import torch

def _softmax(x, axis=-1):
    return torch.softmax(x, dim=axis)
def _gelu(x):
    return torch.nn.functional.gelu(x, approximate="tanh")
def _silu(x):
    return torch.nn.functional.silu(x)
def _sigmoid(x):
    return torch.sigmoid(x)
def _layernorm(x, axis=-1, eps=1e-5):
    m = x.mean(dim=axis, keepdim=True)
    v = x.var(dim=axis, keepdim=True, unbiased=False)
    return (x - m) / torch.sqrt(v + eps)
def _scatter_add(tgt, idx, src, axis=0):
    return tgt.clone().index_add_(axis, idx, src)
def _segment_sum(src, seg, n):
    out = torch.zeros((n,) + src.shape[1:], dtype=src.dtype)
    return out.index_add_(0, seg, src)
def _pad(x, pw):
    flat = tuple(v for pair in reversed(pw) for v in pair)
    return torch.nn.functional.pad(x, flat)

kernel_name = 'recurrent_scan__spectral_gfnet__swiglu_ffn__prompt'

def setup_inputs():

    _w = (lambda a: (lambda t: t.to(torch.bfloat16) if t.is_floating_point() else t)(torch.as_tensor(a)))
    def _A_setup():
        B, S, D = 1, 2048, 512
        Df = D // 2 + 1
        return {
            "x": _w((lambda *s: torch.randn(*s, dtype=torch.bfloat16) * 0.05)(B, S, D)),
            "w_r": _w((lambda *s: torch.randn(*s, dtype=torch.bfloat16) * 0.1)(S, Df) * 0.02),
            "w_i": _w((lambda *s: torch.randn(*s, dtype=torch.bfloat16) * 0.1)(S, Df) * 0.02),
        }
    _A_in = _A_setup()

    def _B_setup():
        B, S, D, F = 1, 2048, 512, 1792
        return {
            "x": _w((lambda *s: torch.randn(*s, dtype=torch.bfloat16) * 0.05)(B, S, D)),
            "w_gate": _w((lambda *s: torch.randn(*s, dtype=torch.bfloat16) * 0.05)(D, F)),
            "w_up": _w((lambda *s: torch.randn(*s, dtype=torch.bfloat16) * 0.05)(D, F)),
            "w_down": _w((lambda *s: torch.randn(*s, dtype=torch.bfloat16) * 0.05)(F, D)),
        }
    _B_in = _B_setup()

    return {
        "A_x": _A_in["x"],
        "A_w_r": _A_in["w_r"],
        "A_w_i": _A_in["w_i"],
        "B_x": _B_in["x"],
        "B_w_gate": _B_in["w_gate"],
        "B_w_up": _B_in["w_up"],
        "B_w_down": _B_in["w_down"],
    }

def reference(A_x, A_w_r, A_w_i, B_x, B_w_gate, B_w_up, B_w_down):
    def _A_ref(x, w_r, w_i):
        B, S, D = x.shape
        X = (lambda x, axes=(-2,-1): torch.fft.rfft2(x.float(), dim=axes))(x, axes=(-2, -1))
        W = w_r + 1j * w_i
        X = X * W
        y = (lambda x, s, axes=(-2,-1): torch.fft.irfft2(x, s=s, dim=axes).to(torch.bfloat16))(X, (S, D), axes=(-2, -1))
        return y

    def _B_ref(x, w_gate, w_up, w_down):
        g = _silu(x @ w_gate)
        u = x @ w_up
        return (g * u) @ w_down

    x = A_x
    xn = _layernorm(x, axis=-1)
    s = _A_ref(xn, A_w_r, A_w_i)
    g = _silu(_B_ref(xn, B_w_gate, B_w_up, B_w_down))
    return x + s * g
```

```cpp
#include <hip/hip_runtime.h>
#include <hip/hip_bf16.h>
#include <stdint.h>


typedef unsigned short u16;
typedef unsigned int u32;
using bf16x8 = __attribute__((ext_vector_type(8))) short;
using s16x4 = __attribute__((ext_vector_type(4))) short;
using f32x4 = __attribute__((ext_vector_type(4))) float;

constexpr int S_ = 2048, D_ = 512, F_ = 1792, DF_ = 257;

struct TwTab { float v[4096]; };
constexpr double TW_PI = 3.14159265358979323846264338327950288;
constexpr double tw_sin(double x) {
  double x2 = x * x, term = x, sum = x;
  for (int n = 1; n < 14; ++n) { term *= -x2 / ((2 * n) * (2 * n + 1)); sum += term; }
  return sum;
}
constexpr double tw_cos(double x) {
  double x2 = x * x, term = 1.0, sum = 1.0;
  for (int n = 1; n < 14; ++n) { term *= -x2 / ((2 * n - 1) * (2 * n)); sum += term; }
  return sum;
}
constexpr TwTab make_tw() {
  TwTab t{};
  for (int j = 0; j < 2048; ++j) {
    int o = j >> 8, r = j & 255;
    double th = 2.0 * TW_PI * r / 2048.0, ph = 2.0 * TW_PI * (256 - r) / 2048.0;
    double c = 0, s = 0;
    switch (o) {
      case 0: c = tw_cos(th); s = tw_sin(th); break;
      case 1: c = tw_sin(ph); s = tw_cos(ph); break;
      case 2: c = -tw_sin(th); s = tw_cos(th); break;
      case 3: c = -tw_cos(ph); s = tw_sin(ph); break;
      case 4: c = -tw_cos(th); s = -tw_sin(th); break;
      case 5: c = -tw_sin(ph); s = -tw_cos(ph); break;
      case 6: c = tw_sin(th); s = -tw_cos(th); break;
      default: c = tw_cos(ph); s = -tw_sin(ph); break;
    }
    t.v[2 * j] = (float)c;
    t.v[2 * j + 1] = (float)(-s);
  }
  return t;
}
__device__ __attribute__((aligned(16))) constexpr TwTab TWT = make_tw();

__device__ __forceinline__ float2 tw(int j) { return reinterpret_cast<const float2*>(TWT.v)[j]; }

__device__ __forceinline__ float bf2f(u16 v) { return __uint_as_float(((u32)v) << 16); }
__device__ __forceinline__ float bflo(u32 v) { return __uint_as_float(v << 16); }
__device__ __forceinline__ float bfhi(u32 v) { return __uint_as_float(v & 0xffff0000u); }
__device__ __forceinline__ u16 f2bf(float f) { return __builtin_bit_cast(u16, (__bf16)f); }
__device__ __forceinline__ float rbf(float f) { return bf2f(f2bf(f)); }
__device__ __forceinline__ u32 pack2(float lo, float hi) { return (u32)f2bf(lo) | ((u32)f2bf(hi) << 16); }

__device__ __forceinline__ float2 cmul(float2 a, float2 b) {
  return make_float2(fmaf(a.x, b.x, -a.y * b.y), fmaf(a.x, b.y, a.y * b.x));
}
__device__ __forceinline__ float2 cmulc(float2 a, float2 b) {
  return make_float2(fmaf(a.x, b.x, a.y * b.y), fmaf(a.y, b.x, -a.x * b.y));
}
__device__ __forceinline__ float2 cadd(float2 a, float2 b) { return make_float2(a.x + b.x, a.y + b.y); }
__device__ __forceinline__ float2 csub(float2 a, float2 b) { return make_float2(a.x - b.x, a.y - b.y); }

template <bool INV>
__device__ __forceinline__ void bfly4(float2& a0, float2& a1, float2& a2, float2& a3) {
  float2 t0 = cadd(a0, a2), t1 = csub(a0, a2), t2 = cadd(a1, a3), d = csub(a1, a3);
  float2 t3 = INV ? make_float2(-d.y, d.x) : make_float2(d.y, -d.x);
  a0 = cadd(t0, t2); a1 = cadd(t1, t3); a2 = csub(t0, t2); a3 = csub(t1, t3);
}
template <bool INV>
__device__ __forceinline__ void bfly8(float2* v) {
  float2 e0 = v[0], e1 = v[2], e2 = v[4], e3 = v[6];
  float2 o0 = v[1], o1 = v[3], o2 = v[5], o3 = v[7];
  bfly4<INV>(e0, e1, e2, e3);
  bfly4<INV>(o0, o1, o2, o3);
  const float h = 0.70710678118654752440f;
  float2 p1, p2, p3;
  if (!INV) {
    p1 = make_float2((o1.x + o1.y) * h, (o1.y - o1.x) * h);
    p2 = make_float2(o2.y, -o2.x);
    p3 = make_float2((o3.y - o3.x) * h, -(o3.x + o3.y) * h);
  } else {
    p1 = make_float2((o1.x - o1.y) * h, (o1.x + o1.y) * h);
    p2 = make_float2(-o2.y, o2.x);
    p3 = make_float2(-(o3.x + o3.y) * h, (o3.x - o3.y) * h);
  }
  v[0] = cadd(e0, o0); v[4] = csub(e0, o0);
  v[1] = cadd(e1, p1); v[5] = csub(e1, p1);
  v[2] = cadd(e2, p2); v[6] = csub(e2, p2);
  v[3] = cadd(e3, p3); v[7] = csub(e3, p3);
}

__device__ __forceinline__ float wave_sum(float v) {
#pragma unroll
  for (int o = 32; o >= 1; o >>= 1) v += __shfl_xor(v, o, 64);
  return v;
}

__host__ __device__ __forceinline__ int wslot(int j) {
  int q1 = j & 7, q2 = (j >> 3) & 7, q3 = (j >> 6) & 7, q4 = j >> 9;
  int u = q1 * 64 + q2 * 8 + q3;
  return ((u >> 8) * 4 + q4) * 256 + (u & 255);
}

constexpr int K0_ROWBLK = 128, K0_WTBLK = 32;
__global__ __launch_bounds__(512) void k0_ln_rowfft(const u16* __restrict__ x, u16* __restrict__ xn,
                                                    float2* __restrict__ XT, const u16* __restrict__ wr,
                                                    const u16* __restrict__ wi, u32* __restrict__ WT,
                                                    float2* __restrict__ W0h, float2* __restrict__ W256h) {
  __shared__ __attribute__((aligned(16))) float2 sm[2 * 16 * 260];
  const int tid = threadIdx.x, lane = tid & 63, wv = tid >> 6;
  const int b = blockIdx.x;
  if (b >= K0_ROWBLK) {
    int a = b - K0_ROWBLK;
    if (a < K0_WTBLK) {
      u32* tile = reinterpret_cast<u32*>(sm);
      int q1 = a & 7, q4 = a >> 3;
      for (int e = tid; e < 64 * 256; e += 512) {
        int i = e >> 8, k = e & 255;
        int j = q1 + 512 * q4 + 8 * i;
        u32 re = wr[j * DF_ + k], im = wi[j * DF_ + k];
        tile[i * 260 + k] = re | (im << 16);
      }
      __syncthreads();
      int base = ((q1 >> 2) * 4 + q4) * 256 + (q1 & 3) * 64;
      for (int e = tid; e < 64 * 256; e += 512) {
        int k = e >> 6, o = e & 63;
        int q2 = o >> 3, q3 = o & 7;
        int i = q2 + 8 * q3;
        if (k > 0) WT[k * 2048 + base + o] = tile[i * 260 + k];
      }
    } else {
      for (int j = tid; j < 2048; j += 512) {
        int jm = (2048 - j) & 2047;
        float a0r = bf2f(wr[j * DF_]), a0i = bf2f(wi[j * DF_]);
        float b0r = bf2f(wr[jm * DF_]), b0i = bf2f(wi[jm * DF_]);
        float a1r = bf2f(wr[j * DF_ + 256]), a1i = bf2f(wi[j * DF_ + 256]);
        float b1r = bf2f(wr[jm * DF_ + 256]), b1i = bf2f(wi[jm * DF_ + 256]);
        int sl = wslot(j);
        W0h[sl] = make_float2(0.5f * (a0r + b0r), 0.5f * (a0i - b0i));
        W256h[sl] = make_float2(0.5f * (a1r + b1r), 0.5f * (a1i - b1i));
      }
    }
    return;
  }
  float2* ex = sm;
  float2* ot = sm + 16 * 260;
  float2 z[2][4];
#pragma unroll
  for (int rr = 0; rr < 2; ++rr) {
    const int row = b * 16 + wv + 8 * rr;
    const u32* xr = reinterpret_cast<const u32*>(x + row * D_);
    u32 v[4];
#pragma unroll
    for (int m = 0; m < 4; ++m) v[m] = xr[lane + 64 * m];
    float f[8];
    float sum = 0.f;
#pragma unroll
    for (int m = 0; m < 4; ++m) { f[2 * m] = bflo(v[m]); f[2 * m + 1] = bfhi(v[m]); sum += f[2 * m] + f[2 * m + 1]; }
    sum = wave_sum(sum);
    float mean = sum * (1.0f / 512.0f);
    float sq = 0.f;
#pragma unroll
    for (int e = 0; e < 8; ++e) { float d = f[e] - mean; sq = fmaf(d, d, sq); }
    sq = wave_sum(sq);
    float var = sq * (1.0f / 512.0f);
    float mb = rbf(mean), vb = rbf(var);
    float den = rbf(sqrtf(rbf(vb + 1e-5f)));
    u32* xo = reinterpret_cast<u32*>(xn + row * D_);
#pragma unroll
    for (int m = 0; m < 4; ++m) {
      float a = rbf(rbf(f[2 * m] - mb) / den), c = rbf(rbf(f[2 * m + 1] - mb) / den);
      xo[lane + 64 * m] = pack2(a, c);
      z[rr][m] = make_float2(a, c);
    }
  }
  {
    float2 w1 = tw(8 * lane), w2 = tw(16 * lane), w3 = tw(24 * lane);
#pragma unroll
    for (int rr = 0; rr < 2; ++rr) {
      bfly4<false>(z[rr][0], z[rr][1], z[rr][2], z[rr][3]);
      z[rr][1] = cmul(z[rr][1], w1); z[rr][2] = cmul(z[rr][2], w2); z[rr][3] = cmul(z[rr][3], w3);
      float2* e = ex + (wv + 8 * rr) * 260;
#pragma unroll
      for (int q = 0; q < 4; ++q) e[q * 64 + lane] = z[rr][q];
    }
  }
  __syncthreads();
  {
    int q1 = lane >> 4, n1 = lane & 15;
    float2 w1 = tw(32 * n1), w2 = tw(64 * n1), w3 = tw(96 * n1);
#pragma unroll
    for (int rr = 0; rr < 2; ++rr) {
      float2* e = ex + (wv + 8 * rr) * 260;
#pragma unroll
      for (int m = 0; m < 4; ++m) z[rr][m] = e[q1 * 64 + n1 + 16 * m];
      bfly4<false>(z[rr][0], z[rr][1], z[rr][2], z[rr][3]);
      z[rr][1] = cmul(z[rr][1], w1); z[rr][2] = cmul(z[rr][2], w2); z[rr][3] = cmul(z[rr][3], w3);
    }
    __syncthreads();
#pragma unroll
    for (int rr = 0; rr < 2; ++rr) {
      float2* e = ex + (wv + 8 * rr) * 260;
#pragma unroll
      for (int q = 0; q < 4; ++q) e[q1 * 64 + q * 16 + n1] = z[rr][q];
    }
  }
  __syncthreads();
  {
    int q12 = lane >> 2, n2 = lane & 3;
    float2 w1 = tw(128 * n2), w2 = tw(256 * n2), w3 = tw(384 * n2);
#pragma unroll
    for (int rr = 0; rr < 2; ++rr) {
      float2* e = ex + (wv + 8 * rr) * 260;
#pragma unroll
      for (int m = 0; m < 4; ++m) z[rr][m] = e[q12 * 16 + n2 + 4 * m];
      bfly4<false>(z[rr][0], z[rr][1], z[rr][2], z[rr][3]);
      z[rr][1] = cmul(z[rr][1], w1); z[rr][2] = cmul(z[rr][2], w2); z[rr][3] = cmul(z[rr][3], w3);
    }
    __syncthreads();
#pragma unroll
    for (int rr = 0; rr < 2; ++rr) {
      float2* e = ex + (wv + 8 * rr) * 260;
#pragma unroll
      for (int q = 0; q < 4; ++q) e[q12 * 16 + q * 4 + n2] = z[rr][q];
    }
  }
  __syncthreads();
  {
    int q1 = lane >> 4, q2 = (lane >> 2) & 3, q3 = lane & 3;
    int kb = q1 + 4 * q2 + 16 * q3;
#pragma unroll
    for (int rr = 0; rr < 2; ++rr) {
      float2* e = ex + (wv + 8 * rr) * 260;
#pragma unroll
      for (int m = 0; m < 4; ++m) z[rr][m] = e[lane * 4 + m];
      bfly4<false>(z[rr][0], z[rr][1], z[rr][2], z[rr][3]);
    }
    __syncthreads();
#pragma unroll
    for (int rr = 0; rr < 2; ++rr) {
      float2* e = ex + (wv + 8 * rr) * 260;
#pragma unroll
      for (int q = 0; q < 4; ++q) e[kb + 64 * q] = z[rr][q];
    }
  }
  __syncthreads();
  {
#pragma unroll
    for (int i = 0; i < 4; ++i) {
      int k = lane + 64 * i;
      float2 w = tw(4 * k);
#pragma unroll
      for (int rr = 0; rr < 2; ++rr) {
        int r16 = wv + 8 * rr;
        const float2* e = ex + r16 * 260;
        float2 zk = e[k], zm = e[(256 - k) & 255];
        float2 E = make_float2(0.5f * (zk.x + zm.x), 0.5f * (zk.y - zm.y));
        float2 Dd = make_float2(zk.x - zm.x, zk.y + zm.y);
        float2 O = make_float2(0.5f * Dd.y, -0.5f * Dd.x);
        float2 X = cadd(E, cmul(O, w));
        if (k == 0) X = make_float2(zk.x + zk.y, zk.x - zk.y);
        ot[k * 16 + (r16 ^ (k & 15))] = X;
      }
    }
  }
  __syncthreads();
  {
    int k = tid >> 1, hf = tid & 1;
    float4* dst = reinterpret_cast<float4*>(XT + (size_t)b * 4096 + k * 16 + hf * 8);
#pragma unroll
    for (int j = 0; j < 4; ++j) {
      int r0 = hf * 8 + 2 * j;
      float2 a = ot[k * 16 + (r0 ^ (k & 15))], c = ot[k * 16 + ((r0 + 1) ^ (k & 15))];
      dst[j] = make_float4(a.x, a.y, c.x, c.y);
    }
  }
}

__device__ __forceinline__ void colfft_block(int k, const float2* __restrict__ XT, float2* __restrict__ YT,
                                             const u32* __restrict__ WT, const float2* __restrict__ W0h,
                                             const float2* __restrict__ W256h, float2* lds) {
  float2* bufA = lds;
  float2* bufB = lds + 2304;
  const int t = threadIdx.x;
  float2 v[8];
#pragma unroll
  for (int m = 0; m < 8; ++m) {
    int s = t + 256 * m;
    v[m] = XT[(size_t)(s >> 4) * 4096 + k * 16 + (s & 15)];
  }
  float2 tw1[7], tw2[7], tw3[7];
  const int q1 = t >> 5, n1 = t & 31, n2 = t & 3, q2r = (t >> 2) & 7;
#pragma unroll
  for (int q = 1; q < 8; ++q) {
    tw1[q - 1] = tw((t * q) & 2047);
    tw2[q - 1] = tw((8 * n1 * q) & 2047);
    tw3[q - 1] = tw((64 * n2 * q) & 2047);
  }
  bfly8<false>(v);
#pragma unroll
  for (int q = 1; q < 8; ++q) v[q] = cmul(v[q], tw1[q - 1]);
#pragma unroll
  for (int q = 0; q < 8; ++q) bufA[q * 256 + t] = v[q];
  __syncthreads();
#pragma unroll
  for (int m = 0; m < 8; ++m) v[m] = bufA[q1 * 256 + n1 + 32 * m];
  bfly8<false>(v);
#pragma unroll
  for (int q = 1; q < 8; ++q) v[q] = cmul(v[q], tw2[q - 1]);
#pragma unroll
  for (int q = 0; q < 8; ++q) bufB[q1 * 288 + q * 36 + n1] = v[q];
  __syncthreads();
#pragma unroll
  for (int m = 0; m < 8; ++m) v[m] = bufB[q1 * 288 + q2r * 36 + n2 + 4 * m];
  bfly8<false>(v);
#pragma unroll
  for (int q = 1; q < 8; ++q) v[q] = cmul(v[q], tw3[q - 1]);
#pragma unroll
  for (int q = 0; q < 8; ++q) bufA[(q1 * 8 + q2r) * 36 + q * 4 + n2] = v[q];
  __syncthreads();
#pragma unroll
  for (int h = 0; h < 2; ++h) {
    int u = t + 256 * h;
#pragma unroll
    for (int m = 0; m < 4; ++m) v[4 * h + m] = bufA[(u >> 3) * 36 + (u & 7) * 4 + m];
  }
  bfly4<false>(v[0], v[1], v[2], v[3]);
  bfly4<false>(v[4], v[5], v[6], v[7]);
  const float scale = 1.0f / (2048.0f * 512.0f);
  if (k != 0) {
#pragma unroll
    for (int i = 0; i < 8; ++i) {
      u32 w = WT[k * 2048 + i * 256 + t];
      float2 wf = make_float2(bflo(w) * scale, bfhi(w) * scale);
      v[i] = cmul(v[i], wf);
    }
  } else {
    int jj[8];
#pragma unroll
    for (int i = 0; i < 8; ++i) {
      int u = t + 256 * (i >> 2), q4 = i & 3;
      int a1 = u >> 6, a2 = (u >> 3) & 7, a3 = u & 7;
      jj[i] = a1 + 8 * a2 + 64 * a3 + 512 * q4;
      bufB[jj[i]] = v[i];
    }
    __syncthreads();
#pragma unroll
    for (int i = 0; i < 8; ++i) {
      float2 zk = v[i], zm = bufB[(2048 - jj[i]) & 2047];
      float2 A = make_float2(0.5f * (zk.x + zm.x), 0.5f * (zk.y - zm.y));
      float2 Dd = make_float2(zk.x - zm.x, zk.y + zm.y);
      float2 Bc = make_float2(0.5f * Dd.y, -0.5f * Dd.x);
      float2 w0 = W0h[i * 256 + t], w1 = W256h[i * 256 + t];
      float2 pa = cmul(A, w0), pb = cmul(Bc, w1);
      v[i] = make_float2((pa.x - pb.y) * scale, (pa.y + pb.x) * scale);
    }
    __syncthreads();
  }
  bfly4<true>(v[0], v[1], v[2], v[3]);
  bfly4<true>(v[4], v[5], v[6], v[7]);
#pragma unroll
  for (int h = 0; h < 2; ++h) {
    int u = t + 256 * h;
#pragma unroll
    for (int m = 0; m < 4; ++m) bufB[(u >> 3) * 36 + (u & 7) * 4 + m] = v[4 * h + m];
  }
  __syncthreads();
#pragma unroll
  for (int q = 0; q < 8; ++q) v[q] = bufB[(q1 * 8 + q2r) * 36 + q * 4 + n2];
#pragma unroll
  for (int q = 1; q < 8; ++q) v[q] = cmulc(v[q], tw3[q - 1]);
  bfly8<true>(v);
#pragma unroll
  for (int m = 0; m < 8; ++m) bufA[q1 * 288 + q2r * 36 + n2 + 4 * m] = v[m];
  __syncthreads();
#pragma unroll
  for (int q = 0; q < 8; ++q) v[q] = bufA[q1 * 288 + q * 36 + n1];
#pragma unroll
  for (int q = 1; q < 8; ++q) v[q] = cmulc(v[q], tw2[q - 1]);
  bfly8<true>(v);
#pragma unroll
  for (int m = 0; m < 8; ++m) bufB[q1 * 256 + n1 + 32 * m] = v[m];
  __syncthreads();
#pragma unroll
  for (int q = 0; q < 8; ++q) v[q] = bufB[q * 256 + t];
#pragma unroll
  for (int q = 1; q < 8; ++q) v[q] = cmulc(v[q], tw1[q - 1]);
  bfly8<true>(v);
#pragma unroll
  for (int m = 0; m < 8; ++m) {
    int s = t + 256 * m;
    YT[(size_t)(s >> 4) * 4096 + k * 16 + (s & 15)] = v[m];
  }
}

__global__ __launch_bounds__(256) void k1_colfft(const float2* __restrict__ XT, float2* __restrict__ YT,
                                                 const u32* __restrict__ WT, const float2* __restrict__ W0h,
                                                 const float2* __restrict__ W256h) {
  __shared__ __attribute__((aligned(16))) float2 lds[2 * 2304];
  colfft_block(blockIdx.x, XT, YT, WT, W0h, W256h, lds);
}

__device__ __forceinline__ void rowifft_block(int b, const float2* __restrict__ YT, u16* __restrict__ sout, float2* lds) {
  float2* it = lds;
  float2* ex = lds + 4096;
  const int tid = threadIdx.x, lane = tid & 63, wv = tid >> 6;
  {
    const float4* src = reinterpret_cast<const float4*>(YT + (size_t)b * 4096);
#pragma unroll
    for (int j = 0; j < 8; ++j) {
      int e2 = tid + 256 * j;
      float4 d = src[e2];
      int k = e2 >> 3, r0 = (e2 & 7) * 2;
      it[k * 16 + (r0 ^ (k & 15))] = make_float2(d.x, d.y);
      it[k * 16 + ((r0 + 1) ^ (k & 15))] = make_float2(d.z, d.w);
    }
  }
  __syncthreads();
  float2 z[4][4];
  {
    int q1 = lane >> 4, q2 = (lane >> 2) & 3, q3 = lane & 3;
    int kb = q1 + 4 * q2 + 16 * q3;
#pragma unroll
    for (int q = 0; q < 4; ++q) {
      int k = kb + 64 * q, km = (256 - k) & 255;
      float2 w = tw(4 * k);
#pragma unroll
      for (int rr = 0; rr < 4; ++rr) {
        int r16 = wv + 4 * rr;
        float2 xk = it[k * 16 + (r16 ^ (k & 15))], xm = it[km * 16 + (r16 ^ (km & 15))];
        float2 Z;
        if (k == 0) {
          Z = make_float2(0.5f * (xk.x + xk.y), 0.5f * (xk.x - xk.y));
        } else {
          float2 E = make_float2(0.5f * (xk.x + xm.x), 0.5f * (xk.y - xm.y));
          float2 Dd = make_float2(0.5f * (xk.x - xm.x), 0.5f * (xk.y + xm.y));
          float2 O = cmulc(Dd, w);
          Z = make_float2(E.x - O.y, E.y + O.x);
        }
        z[rr][q] = Z;
      }
    }
  }
#pragma unroll
  for (int rr = 0; rr < 4; ++rr) {
    bfly4<true>(z[rr][0], z[rr][1], z[rr][2], z[rr][3]);
    float2* e = ex + (wv + 4 * rr) * 260;
#pragma unroll
    for (int m = 0; m < 4; ++m) e[lane * 4 + m] = z[rr][m];
  }
  __syncthreads();
  {
    int q12 = lane >> 2, n2 = lane & 3;
    float2 w1 = tw(128 * n2), w2 = tw(256 * n2), w3 = tw(384 * n2);
#pragma unroll
    for (int rr = 0; rr < 4; ++rr) {
      float2* e = ex + (wv + 4 * rr) * 260;
#pragma unroll
      for (int q = 0; q < 4; ++q) z[rr][q] = e[q12 * 16 + q * 4 + n2];
      z[rr][1] = cmulc(z[rr][1], w1); z[rr][2] = cmulc(z[rr][2], w2); z[rr][3] = cmulc(z[rr][3], w3);
      bfly4<true>(z[rr][0], z[rr][1], z[rr][2], z[rr][3]);
    }
    __syncthreads();
#pragma unroll
    for (int rr = 0; rr < 4; ++rr) {
      float2* e = ex + (wv + 4 * rr) * 260;
#pragma unroll
      for (int m = 0; m < 4; ++m) e[q12 * 16 + n2 + 4 * m] = z[rr][m];
    }
  }
  __syncthreads();
  {
    int q1 = lane >> 4, n1 = lane & 15;
    float2 w1 = tw(32 * n1), w2 = tw(64 * n1), w3 = tw(96 * n1);
#pragma unroll
    for (int rr = 0; rr < 4; ++rr) {
      float2* e = ex + (wv + 4 * rr) * 260;
#pragma unroll
      for (int q = 0; q < 4; ++q) z[rr][q] = e[q1 * 64 + q * 16 + n1];
      z[rr][1] = cmulc(z[rr][1], w1); z[rr][2] = cmulc(z[rr][2], w2); z[rr][3] = cmulc(z[rr][3], w3);
      bfly4<true>(z[rr][0], z[rr][1], z[rr][2], z[rr][3]);
    }
    __syncthreads();
#pragma unroll
    for (int rr = 0; rr < 4; ++rr) {
      float2* e = ex + (wv + 4 * rr) * 260;
#pragma unroll
      for (int m = 0; m < 4; ++m) e[q1 * 64 + n1 + 16 * m] = z[rr][m];
    }
  }
  __syncthreads();
  {
    float2 w1 = tw(8 * lane), w2 = tw(16 * lane), w3 = tw(24 * lane);
#pragma unroll
    for (int rr = 0; rr < 4; ++rr) {
      float2* e = ex + (wv + 4 * rr) * 260;
#pragma unroll
      for (int q = 0; q < 4; ++q) z[rr][q] = e[q * 64 + lane];
      z[rr][1] = cmulc(z[rr][1], w1); z[rr][2] = cmulc(z[rr][2], w2); z[rr][3] = cmulc(z[rr][3], w3);
      bfly4<true>(z[rr][0], z[rr][1], z[rr][2], z[rr][3]);
      const int row = b * 16 + wv + 4 * rr;
      u32* so = reinterpret_cast<u32*>(sout + row * D_);
#pragma unroll
      for (int m = 0; m < 4; ++m) so[lane + 64 * m] = pack2(2.0f * z[rr][m].x, 2.0f * z[rr][m].y);
    }
  }
}

__global__ __launch_bounds__(256) void k2_rowifft(const float2* __restrict__ YT, u16* __restrict__ sout) {
  __shared__ __attribute__((aligned(16))) float2 lds[4096 + 16 * 260];
  rowifft_block(blockIdx.x, YT, sout, lds);
}

__device__ __forceinline__ float silu_f(float v) { return v / (1.0f + __expf(-v)); }

__global__ __launch_bounds__(256) void g1_simple(const u16* __restrict__ xn, const u16* __restrict__ wg,
                                                 const u16* __restrict__ wu, u16* __restrict__ h) {
  __shared__ __attribute__((aligned(16))) u16 As[64][40];
  __shared__ __attribute__((aligned(16))) u16 Bg[64][40];
  __shared__ __attribute__((aligned(16))) u16 Bu[64][40];
  const int tid = threadIdx.x, lane = tid & 63, wv = tid >> 6;
  const int m0 = blockIdx.y * 64, n0 = blockIdx.x * 64;
  const int wm = wv >> 1, wn = wv & 1;
  f32x4 accg[2][2] = {}, accu[2][2] = {};
  for (int k0 = 0; k0 < D_; k0 += 32) {
    for (int e = tid; e < 64 * 32; e += 256) {
      int r = e >> 5, c = e & 31;
      As[r][c] = xn[(m0 + r) * D_ + k0 + c];
      int kk = e >> 6, nn = e & 63;
      Bg[nn][kk] = wg[(k0 + kk) * F_ + n0 + nn];
      Bu[nn][kk] = wu[(k0 + kk) * F_ + n0 + nn];
    }
    __syncthreads();
#pragma unroll
    for (int mi = 0; mi < 2; ++mi) {
      bf16x8 a = *reinterpret_cast<const bf16x8*>(&As[wm * 32 + mi * 16 + (lane & 15)][(lane >> 4) * 8]);
#pragma unroll
      for (int ni = 0; ni < 2; ++ni) {
        bf16x8 bg = *reinterpret_cast<const bf16x8*>(&Bg[wn * 32 + ni * 16 + (lane & 15)][(lane >> 4) * 8]);
        bf16x8 bu = *reinterpret_cast<const bf16x8*>(&Bu[wn * 32 + ni * 16 + (lane & 15)][(lane >> 4) * 8]);
        accg[mi][ni] = __builtin_amdgcn_mfma_f32_16x16x32_bf16(a, bg, accg[mi][ni], 0, 0, 0);
        accu[mi][ni] = __builtin_amdgcn_mfma_f32_16x16x32_bf16(a, bu, accu[mi][ni], 0, 0, 0);
      }
    }
    __syncthreads();
  }
#pragma unroll
  for (int mi = 0; mi < 2; ++mi)
#pragma unroll
    for (int ni = 0; ni < 2; ++ni)
#pragma unroll
      for (int j = 0; j < 4; ++j) {
        int row = m0 + wm * 32 + mi * 16 + (lane >> 4) * 4 + j, col = n0 + wn * 32 + ni * 16 + (lane & 15);
        float g = rbf(accg[mi][ni][j]), u = rbf(accu[mi][ni][j]);
        float sg = rbf(silu_f(g));
        h[row * F_ + col] = f2bf(sg * u);
      }
}

__global__ __launch_bounds__(256) void g2_simple(const u16* __restrict__ h, const u16* __restrict__ wd,
                                                 u16* __restrict__ gout) {
  __shared__ __attribute__((aligned(16))) u16 As[64][40];
  __shared__ __attribute__((aligned(16))) u16 Bs[64][40];
  const int tid = threadIdx.x, lane = tid & 63, wv = tid >> 6;
  const int m0 = blockIdx.y * 64, n0 = blockIdx.x * 64;
  const int wm = wv >> 1, wn = wv & 1;
  f32x4 acc[2][2] = {};
  for (int k0 = 0; k0 < F_; k0 += 32) {
    for (int e = tid; e < 64 * 32; e += 256) {
      int r = e >> 5, c = e & 31;
      As[r][c] = h[(m0 + r) * F_ + k0 + c];
      int kk = e >> 6, nn = e & 63;
      Bs[nn][kk] = wd[(k0 + kk) * D_ + n0 + nn];
    }
    __syncthreads();
#pragma unroll
    for (int mi = 0; mi < 2; ++mi) {
      bf16x8 a = *reinterpret_cast<const bf16x8*>(&As[wm * 32 + mi * 16 + (lane & 15)][(lane >> 4) * 8]);
#pragma unroll
      for (int ni = 0; ni < 2; ++ni) {
        bf16x8 bb = *reinterpret_cast<const bf16x8*>(&Bs[wn * 32 + ni * 16 + (lane & 15)][(lane >> 4) * 8]);
        acc[mi][ni] = __builtin_amdgcn_mfma_f32_16x16x32_bf16(a, bb, acc[mi][ni], 0, 0, 0);
      }
    }
    __syncthreads();
  }
#pragma unroll
  for (int mi = 0; mi < 2; ++mi)
#pragma unroll
    for (int ni = 0; ni < 2; ++ni)
#pragma unroll
      for (int j = 0; j < 4; ++j) {
        int row = m0 + wm * 32 + mi * 16 + (lane >> 4) * 4 + j, col = n0 + wn * 32 + ni * 16 + (lane & 15);
        float f = rbf(acc[mi][ni][j]);
        gout[row * D_ + col] = f2bf(silu_f(f));
      }
}

__global__ __launch_bounds__(256) void k3_combine(const u16* __restrict__ x, const u16* __restrict__ s,
                                                  const u16* __restrict__ g, u16* __restrict__ out) {
  int i = blockIdx.x * 256 + threadIdx.x;
  uint4 xv = reinterpret_cast<const uint4*>(x)[i], sv = reinterpret_cast<const uint4*>(s)[i],
        gv = reinterpret_cast<const uint4*>(g)[i];
  u32 xa[4] = {xv.x, xv.y, xv.z, xv.w}, sa[4] = {sv.x, sv.y, sv.z, sv.w}, ga[4] = {gv.x, gv.y, gv.z, gv.w}, o[4];
#pragma unroll
  for (int j = 0; j < 4; ++j) {
    float p0 = rbf(bflo(sa[j]) * bflo(ga[j])), p1 = rbf(bfhi(sa[j]) * bfhi(ga[j]));
    o[j] = pack2(bflo(xa[j]) + p0, bfhi(xa[j]) + p1);
  }
  reinterpret_cast<uint4*>(out)[i] = make_uint4(o[0], o[1], o[2], o[3]);
}

extern "C" void kernel_launch(void* const* d_in, const int* in_sizes, int n_in, void* d_out, int out_size,
                              void* d_ws, size_t ws_size, hipStream_t stream) {
  const u16* x = (const u16*)d_in[0];
  const u16* wr = (const u16*)d_in[1];
  const u16* wi = (const u16*)d_in[2];
  const u16* wg = (const u16*)d_in[4];
  const u16* wu = (const u16*)d_in[5];
  const u16* wd = (const u16*)d_in[6];
  u16* out = (u16*)d_out;
  char* ws = (char*)d_ws;
  u16* xn = (u16*)(ws + 0);
  float2* XT = (float2*)(ws + (2u << 20));
  float2* YT = (float2*)(ws + (6u << 20));
  u32* WT = (u32*)(ws + (10u << 20));
  float2* W0h = (float2*)(ws + (12u << 20));
  float2* W256h = (float2*)(ws + (12u << 20) + 65536);
  u16* h = (u16*)(ws + (13u << 20));
  u16* g = (u16*)(ws + (21u << 20));
  u16* sb = (u16*)(ws + (23u << 20));

  k0_ln_rowfft<<<K0_ROWBLK + K0_WTBLK + 1, 512, 0, stream>>>(x, xn, XT, wr, wi, WT, W0h, W256h);
  g1_simple<<<dim3(F_ / 64, S_ / 64), 256, 0, stream>>>(xn, wg, wu, h);
  k1_colfft<<<256, 256, 0, stream>>>(XT, YT, WT, W0h, W256h);
  g2_simple<<<dim3(D_ / 64, S_ / 64), 256, 0, stream>>>(h, wd, g);
  k2_rowifft<<<128, 256, 0, stream>>>(YT, sb);
  k3_combine<<<S_ * D_ / 8 / 256, 256, 0, stream>>>(x, sb, g, out);
}
```

```cpp
#include <hip/hip_runtime.h>
#include <hip/hip_bf16.h>
#include <stdint.h>


typedef unsigned short u16;
typedef unsigned int u32;
using bf16x8 = __attribute__((ext_vector_type(8))) short;
using s16x4 = __attribute__((ext_vector_type(4))) short;
using f32x4 = __attribute__((ext_vector_type(4))) float;

constexpr int S_ = 2048, D_ = 512, F_ = 1792, DF_ = 257;

struct TwTab { float v[4096]; };
constexpr double TW_PI = 3.14159265358979323846264338327950288;
constexpr double tw_sin(double x) {
  double x2 = x * x, term = x, sum = x;
  for (int n = 1; n < 14; ++n) { term *= -x2 / ((2 * n) * (2 * n + 1)); sum += term; }
  return sum;
}
constexpr double tw_cos(double x) {
  double x2 = x * x, term = 1.0, sum = 1.0;
  for (int n = 1; n < 14; ++n) { term *= -x2 / ((2 * n - 1) * (2 * n)); sum += term; }
  return sum;
}
constexpr TwTab make_tw() {
  TwTab t{};
  for (int j = 0; j < 2048; ++j) {
    int o = j >> 8, r = j & 255;
    double th = 2.0 * TW_PI * r / 2048.0, ph = 2.0 * TW_PI * (256 - r) / 2048.0;
    double c = 0, s = 0;
    switch (o) {
      case 0: c = tw_cos(th); s = tw_sin(th); break;
      case 1: c = tw_sin(ph); s = tw_cos(ph); break;
      case 2: c = -tw_sin(th); s = tw_cos(th); break;
      case 3: c = -tw_cos(ph); s = tw_sin(ph); break;
      case 4: c = -tw_cos(th); s = -tw_sin(th); break;
      case 5: c = -tw_sin(ph); s = -tw_cos(ph); break;
      case 6: c = tw_sin(th); s = -tw_cos(th); break;
      default: c = tw_cos(ph); s = -tw_sin(ph); break;
    }
    t.v[2 * j] = (float)c;
    t.v[2 * j + 1] = (float)(-s);
  }
  return t;
}
__device__ __attribute__((aligned(16))) constexpr TwTab TWT = make_tw();

__device__ __forceinline__ float2 tw(int j) { return reinterpret_cast<const float2*>(TWT.v)[j]; }

__device__ __forceinline__ float bf2f(u16 v) { return __uint_as_float(((u32)v) << 16); }
__device__ __forceinline__ float bflo(u32 v) { return __uint_as_float(v << 16); }
__device__ __forceinline__ float bfhi(u32 v) { return __uint_as_float(v & 0xffff0000u); }
__device__ __forceinline__ u16 f2bf(float f) { return __builtin_bit_cast(u16, (__bf16)f); }
__device__ __forceinline__ float rbf(float f) { return bf2f(f2bf(f)); }
__device__ __forceinline__ u32 pack2(float lo, float hi) { return (u32)f2bf(lo) | ((u32)f2bf(hi) << 16); }

__device__ __forceinline__ float2 cmul(float2 a, float2 b) {
  return make_float2(fmaf(a.x, b.x, -a.y * b.y), fmaf(a.x, b.y, a.y * b.x));
}
__device__ __forceinline__ float2 cmulc(float2 a, float2 b) {
  return make_float2(fmaf(a.x, b.x, a.y * b.y), fmaf(a.y, b.x, -a.x * b.y));
}
__device__ __forceinline__ float2 cadd(float2 a, float2 b) { return make_float2(a.x + b.x, a.y + b.y); }
__device__ __forceinline__ float2 csub(float2 a, float2 b) { return make_float2(a.x - b.x, a.y - b.y); }

template <bool INV>
__device__ __forceinline__ void bfly4(float2& a0, float2& a1, float2& a2, float2& a3) {
  float2 t0 = cadd(a0, a2), t1 = csub(a0, a2), t2 = cadd(a1, a3), d = csub(a1, a3);
  float2 t3 = INV ? make_float2(-d.y, d.x) : make_float2(d.y, -d.x);
  a0 = cadd(t0, t2); a1 = cadd(t1, t3); a2 = csub(t0, t2); a3 = csub(t1, t3);
}
template <bool INV>
__device__ __forceinline__ void bfly8(float2* v) {
  float2 e0 = v[0], e1 = v[2], e2 = v[4], e3 = v[6];
  float2 o0 = v[1], o1 = v[3], o2 = v[5], o3 = v[7];
  bfly4<INV>(e0, e1, e2, e3);
  bfly4<INV>(o0, o1, o2, o3);
  const float h = 0.70710678118654752440f;
  float2 p1, p2, p3;
  if (!INV) {
    p1 = make_float2((o1.x + o1.y) * h, (o1.y - o1.x) * h);
    p2 = make_float2(o2.y, -o2.x);
    p3 = make_float2((o3.y - o3.x) * h, -(o3.x + o3.y) * h);
  } else {
    p1 = make_float2((o1.x - o1.y) * h, (o1.x + o1.y) * h);
    p2 = make_float2(-o2.y, o2.x);
    p3 = make_float2(-(o3.x + o3.y) * h, (o3.x - o3.y) * h);
  }
  v[0] = cadd(e0, o0); v[4] = csub(e0, o0);
  v[1] = cadd(e1, p1); v[5] = csub(e1, p1);
  v[2] = cadd(e2, p2); v[6] = csub(e2, p2);
  v[3] = cadd(e3, p3); v[7] = csub(e3, p3);
}

__device__ __forceinline__ float wave_sum(float v) {
#pragma unroll
  for (int o = 32; o >= 1; o >>= 1) v += __shfl_xor(v, o, 64);
  return v;
}

__host__ __device__ __forceinline__ int wslot(int j) {
  int q1 = j & 7, q2 = (j >> 3) & 7, q3 = (j >> 6) & 7, q4 = j >> 9;
  int u = q1 * 64 + q2 * 8 + q3;
  return ((u >> 8) * 4 + q4) * 256 + (u & 255);
}

constexpr int K0_ROWBLK = 128, K0_WTBLK = 32;
__global__ __launch_bounds__(512) void k0_ln_rowfft(const u16* __restrict__ x, u16* __restrict__ xn,
                                                    float2* __restrict__ XT, const u16* __restrict__ wr,
                                                    const u16* __restrict__ wi, u32* __restrict__ WT,
                                                    float2* __restrict__ W0h, float2* __restrict__ W256h) {
  __shared__ __attribute__((aligned(16))) float2 sm[2 * 16 * 260];
  const int tid = threadIdx.x, lane = tid & 63, wv = tid >> 6;
  const int b = blockIdx.x;
  if (b >= K0_ROWBLK) {
    int a = b - K0_ROWBLK;
    if (a < K0_WTBLK) {
      u32* tile = reinterpret_cast<u32*>(sm);
      int q1 = a & 7, q4 = a >> 3;
      for (int e = tid; e < 64 * 256; e += 512) {
        int i = e >> 8, k = e & 255;
        int j = q1 + 512 * q4 + 8 * i;
        u32 re = wr[j * DF_ + k], im = wi[j * DF_ + k];
        tile[i * 260 + k] = re | (im << 16);
      }
      __syncthreads();
      int base = ((q1 >> 2) * 4 + q4) * 256 + (q1 & 3) * 64;
      for (int e = tid; e < 64 * 256; e += 512) {
        int k = e >> 6, o = e & 63;
        int q2 = o >> 3, q3 = o & 7;
        int i = q2 + 8 * q3;
        if (k > 0) WT[k * 2048 + base + o] = tile[i * 260 + k];
      }
    } else {
      for (int j = tid; j < 2048; j += 512) {
        int jm = (2048 - j) & 2047;
        float a0r = bf2f(wr[j * DF_]), a0i = bf2f(wi[j * DF_]);
        float b0r = bf2f(wr[jm * DF_]), b0i = bf2f(wi[jm * DF_]);
        float a1r = bf2f(wr[j * DF_ + 256]), a1i = bf2f(wi[j * DF_ + 256]);
        float b1r = bf2f(wr[jm * DF_ + 256]), b1i = bf2f(wi[jm * DF_ + 256]);
        int sl = wslot(j);
        W0h[sl] = make_float2(0.5f * (a0r + b0r), 0.5f * (a0i - b0i));
        W256h[sl] = make_float2(0.5f * (a1r + b1r), 0.5f * (a1i - b1i));
      }
    }
    return;
  }
  float2* ex = sm;
  float2* ot = sm + 16 * 260;
  float2 z[2][4];
#pragma unroll
  for (int rr = 0; rr < 2; ++rr) {
    const int row = b * 16 + wv + 8 * rr;
    const u32* xr = reinterpret_cast<const u32*>(x + row * D_);
    u32 v[4];
#pragma unroll
    for (int m = 0; m < 4; ++m) v[m] = xr[lane + 64 * m];
    float f[8];
    float sum = 0.f;
#pragma unroll
    for (int m = 0; m < 4; ++m) { f[2 * m] = bflo(v[m]); f[2 * m + 1] = bfhi(v[m]); sum += f[2 * m] + f[2 * m + 1]; }
    sum = wave_sum(sum);
    float mean = sum * (1.0f / 512.0f);
    float sq = 0.f;
#pragma unroll
    for (int e = 0; e < 8; ++e) { float d = f[e] - mean; sq = fmaf(d, d, sq); }
    sq = wave_sum(sq);
    float var = sq * (1.0f / 512.0f);
    float mb = rbf(mean), vb = rbf(var);
    float den = rbf(sqrtf(rbf(vb + 1e-5f)));
    u32* xo = reinterpret_cast<u32*>(xn + row * D_);
#pragma unroll
    for (int m = 0; m < 4; ++m) {
      float a = rbf(rbf(f[2 * m] - mb) / den), c = rbf(rbf(f[2 * m + 1] - mb) / den);
      xo[lane + 64 * m] = pack2(a, c);
      z[rr][m] = make_float2(a, c);
    }
  }
  {
    float2 w1 = tw(8 * lane), w2 = tw(16 * lane), w3 = tw(24 * lane);
#pragma unroll
    for (int rr = 0; rr < 2; ++rr) {
      bfly4<false>(z[rr][0], z[rr][1], z[rr][2], z[rr][3]);
      z[rr][1] = cmul(z[rr][1], w1); z[rr][2] = cmul(z[rr][2], w2); z[rr][3] = cmul(z[rr][3], w3);
      float2* e = ex + (wv + 8 * rr) * 260;
#pragma unroll
      for (int q = 0; q < 4; ++q) e[q * 64 + lane] = z[rr][q];
    }
  }
  __syncthreads();
  {
    int q1 = lane >> 4, n1 = lane & 15;
    float2 w1 = tw(32 * n1), w2 = tw(64 * n1), w3 = tw(96 * n1);
#pragma unroll
    for (int rr = 0; rr < 2; ++rr) {
      float2* e = ex + (wv + 8 * rr) * 260;
#pragma unroll
      for (int m = 0; m < 4; ++m) z[rr][m] = e[q1 * 64 + n1 + 16 * m];
      bfly4<false>(z[rr][0], z[rr][1], z[rr][2], z[rr][3]);
      z[rr][1] = cmul(z[rr][1], w1); z[rr][2] = cmul(z[rr][2], w2); z[rr][3] = cmul(z[rr][3], w3);
    }
    __syncthreads();
#pragma unroll
    for (int rr = 0; rr < 2; ++rr) {
      float2* e = ex + (wv + 8 * rr) * 260;
#pragma unroll
      for (int q = 0; q < 4; ++q) e[q1 * 64 + q * 16 + n1] = z[rr][q];
    }
  }
  __syncthreads();
  {
    int q12 = lane >> 2, n2 = lane & 3;
    float2 w1 = tw(128 * n2), w2 = tw(256 * n2), w3 = tw(384 * n2);
#pragma unroll
    for (int rr = 0; rr < 2; ++rr) {
      float2* e = ex + (wv + 8 * rr) * 260;
#pragma unroll
      for (int m = 0; m < 4; ++m) z[rr][m] = e[q12 * 16 + n2 + 4 * m];
      bfly4<false>(z[rr][0], z[rr][1], z[rr][2], z[rr][3]);
      z[rr][1] = cmul(z[rr][1], w1); z[rr][2] = cmul(z[rr][2], w2); z[rr][3] = cmul(z[rr][3], w3);
    }
    __syncthreads();
#pragma unroll
    for (int rr = 0; rr < 2; ++rr) {
      float2* e = ex + (wv + 8 * rr) * 260;
#pragma unroll
      for (int q = 0; q < 4; ++q) e[q12 * 16 + q * 4 + n2] = z[rr][q];
    }
  }
  __syncthreads();
  {
    int q1 = lane >> 4, q2 = (lane >> 2) & 3, q3 = lane & 3;
    int kb = q1 + 4 * q2 + 16 * q3;
#pragma unroll
    for (int rr = 0; rr < 2; ++rr) {
      float2* e = ex + (wv + 8 * rr) * 260;
#pragma unroll
      for (int m = 0; m < 4; ++m) z[rr][m] = e[lane * 4 + m];
      bfly4<false>(z[rr][0], z[rr][1], z[rr][2], z[rr][3]);
    }
    __syncthreads();
#pragma unroll
    for (int rr = 0; rr < 2; ++rr) {
      float2* e = ex + (wv + 8 * rr) * 260;
#pragma unroll
      for (int q = 0; q < 4; ++q) e[kb + 64 * q] = z[rr][q];
    }
  }
  __syncthreads();
  {
#pragma unroll
    for (int i = 0; i < 4; ++i) {
      int k = lane + 64 * i;
      float2 w = tw(4 * k);
#pragma unroll
      for (int rr = 0; rr < 2; ++rr) {
        int r16 = wv + 8 * rr;
        const float2* e = ex + r16 * 260;
        float2 zk = e[k], zm = e[(256 - k) & 255];
        float2 E = make_float2(0.5f * (zk.x + zm.x), 0.5f * (zk.y - zm.y));
        float2 Dd = make_float2(zk.x - zm.x, zk.y + zm.y);
        float2 O = make_float2(0.5f * Dd.y, -0.5f * Dd.x);
        float2 X = cadd(E, cmul(O, w));
        if (k == 0) X = make_float2(zk.x + zk.y, zk.x - zk.y);
        ot[k * 16 + (r16 ^ (k & 15))] = X;
      }
    }
  }
  __syncthreads();
  {
    int k = tid >> 1, hf = tid & 1;
    float4* dst = reinterpret_cast<float4*>(XT + (size_t)b * 4096 + k * 16 + hf * 8);
#pragma unroll
    for (int j = 0; j < 4; ++j) {
      int r0 = hf * 8 + 2 * j;
      float2 a = ot[k * 16 + (r0 ^ (k & 15))], c = ot[k * 16 + ((r0 + 1) ^ (k & 15))];
      dst[j] = make_float4(a.x, a.y, c.x, c.y);
    }
  }
}

__device__ __forceinline__ void colfft_block(int k, const float2* __restrict__ XT, float2* __restrict__ YT,
                                             const u32* __restrict__ WT, const float2* __restrict__ W0h,
                                             const float2* __restrict__ W256h, float2* lds) {
  float2* bufA = lds;
  float2* bufB = lds + 2304;
  const int t = threadIdx.x;
  float2 v[8];
#pragma unroll
  for (int m = 0; m < 8; ++m) {
    int s = t + 256 * m;
    v[m] = XT[(size_t)(s >> 4) * 4096 + k * 16 + (s & 15)];
  }
  float2 tw1[7], tw2[7], tw3[7];
  const int q1 = t >> 5, n1 = t & 31, n2 = t & 3, q2r = (t >> 2) & 7;
#pragma unroll
  for (int q = 1; q < 8; ++q) {
    tw1[q - 1] = tw((t * q) & 2047);
    tw2[q - 1] = tw((8 * n1 * q) & 2047);
    tw3[q - 1] = tw((64 * n2 * q) & 2047);
  }
  bfly8<false>(v);
#pragma unroll
  for (int q = 1; q < 8; ++q) v[q] = cmul(v[q], tw1[q - 1]);
#pragma unroll
  for (int q = 0; q < 8; ++q) bufA[q * 256 + t] = v[q];
  __syncthreads();
#pragma unroll
  for (int m = 0; m < 8; ++m) v[m] = bufA[q1 * 256 + n1 + 32 * m];
  bfly8<false>(v);
#pragma unroll
  for (int q = 1; q < 8; ++q) v[q] = cmul(v[q], tw2[q - 1]);
#pragma unroll
  for (int q = 0; q < 8; ++q) bufB[q1 * 288 + q * 36 + n1] = v[q];
  __syncthreads();
#pragma unroll
  for (int m = 0; m < 8; ++m) v[m] = bufB[q1 * 288 + q2r * 36 + n2 + 4 * m];
  bfly8<false>(v);
#pragma unroll
  for (int q = 1; q < 8; ++q) v[q] = cmul(v[q], tw3[q - 1]);
#pragma unroll
  for (int q = 0; q < 8; ++q) bufA[(q1 * 8 + q2r) * 36 + q * 4 + n2] = v[q];
  __syncthreads();
#pragma unroll
  for (int h = 0; h < 2; ++h) {
    int u = t + 256 * h;
#pragma unroll
    for (int m = 0; m < 4; ++m) v[4 * h + m] = bufA[(u >> 3) * 36 + (u & 7) * 4 + m];
  }
  bfly4<false>(v[0], v[1], v[2], v[3]);
  bfly4<false>(v[4], v[5], v[6], v[7]);
  const float scale = 1.0f / (2048.0f * 512.0f);
  if (k != 0) {
#pragma unroll
    for (int i = 0; i < 8; ++i) {
      u32 w = WT[k * 2048 + i * 256 + t];
      float2 wf = make_float2(bflo(w) * scale, bfhi(w) * scale);
      v[i] = cmul(v[i], wf);
    }
  } else {
    int jj[8];
#pragma unroll
    for (int i = 0; i < 8; ++i) {
      int u = t + 256 * (i >> 2), q4 = i & 3;
      int a1 = u >> 6, a2 = (u >> 3) & 7, a3 = u & 7;
      jj[i] = a1 + 8 * a2 + 64 * a3 + 512 * q4;
      bufB[jj[i]] = v[i];
    }
    __syncthreads();
#pragma unroll
    for (int i = 0; i < 8; ++i) {
      float2 zk = v[i], zm = bufB[(2048 - jj[i]) & 2047];
      float2 A = make_float2(0.5f * (zk.x + zm.x), 0.5f * (zk.y - zm.y));
      float2 Dd = make_float2(zk.x - zm.x, zk.y + zm.y);
      float2 Bc = make_float2(0.5f * Dd.y, -0.5f * Dd.x);
      float2 w0 = W0h[i * 256 + t], w1 = W256h[i * 256 + t];
      float2 pa = cmul(A, w0), pb = cmul(Bc, w1);
      v[i] = make_float2((pa.x - pb.y) * scale, (pa.y + pb.x) * scale);
    }
    __syncthreads();
  }
  bfly4<true>(v[0], v[1], v[2], v[3]);
  bfly4<true>(v[4], v[5], v[6], v[7]);
#pragma unroll
  for (int h = 0; h < 2; ++h) {
    int u = t + 256 * h;
#pragma unroll
    for (int m = 0; m < 4; ++m) bufB[(u >> 3) * 36 + (u & 7) * 4 + m] = v[4 * h + m];
  }
  __syncthreads();
#pragma unroll
  for (int q = 0; q < 8; ++q) v[q] = bufB[(q1 * 8 + q2r) * 36 + q * 4 + n2];
#pragma unroll
  for (int q = 1; q < 8; ++q) v[q] = cmulc(v[q], tw3[q - 1]);
  bfly8<true>(v);
#pragma unroll
  for (int m = 0; m < 8; ++m) bufA[q1 * 288 + q2r * 36 + n2 + 4 * m] = v[m];
  __syncthreads();
#pragma unroll
  for (int q = 0; q < 8; ++q) v[q] = bufA[q1 * 288 + q * 36 + n1];
#pragma unroll
  for (int q = 1; q < 8; ++q) v[q] = cmulc(v[q], tw2[q - 1]);
  bfly8<true>(v);
#pragma unroll
  for (int m = 0; m < 8; ++m) bufB[q1 * 256 + n1 + 32 * m] = v[m];
  __syncthreads();
#pragma unroll
  for (int q = 0; q < 8; ++q) v[q] = bufB[q * 256 + t];
#pragma unroll
  for (int q = 1; q < 8; ++q) v[q] = cmulc(v[q], tw1[q - 1]);
  bfly8<true>(v);
#pragma unroll
  for (int m = 0; m < 8; ++m) {
    int s = t + 256 * m;
    YT[(size_t)(s >> 4) * 4096 + k * 16 + (s & 15)] = v[m];
  }
}


__device__ __forceinline__ void rowifft_block(int b, const float2* __restrict__ YT, u16* __restrict__ sout, float2* lds,
                                              const u16* __restrict__ xin, const u16* __restrict__ gin) {
  float2* it = lds;
  float2* ex = lds + 4096;
  const int tid = threadIdx.x, lane = tid & 63, wv = tid >> 6;
  {
    const float4* src = reinterpret_cast<const float4*>(YT + (size_t)b * 4096);
#pragma unroll
    for (int j = 0; j < 8; ++j) {
      int e2 = tid + 256 * j;
      float4 d = src[e2];
      int k = e2 >> 3, r0 = (e2 & 7) * 2;
      it[k * 16 + (r0 ^ (k & 15))] = make_float2(d.x, d.y);
      it[k * 16 + ((r0 + 1) ^ (k & 15))] = make_float2(d.z, d.w);
    }
  }
  __syncthreads();
  float2 z[4][4];
  {
    int q1 = lane >> 4, q2 = (lane >> 2) & 3, q3 = lane & 3;
    int kb = q1 + 4 * q2 + 16 * q3;
#pragma unroll
    for (int q = 0; q < 4; ++q) {
      int k = kb + 64 * q, km = (256 - k) & 255;
      float2 w = tw(4 * k);
#pragma unroll
      for (int rr = 0; rr < 4; ++rr) {
        int r16 = wv + 4 * rr;
        float2 xk = it[k * 16 + (r16 ^ (k & 15))], xm = it[km * 16 + (r16 ^ (km & 15))];
        float2 Z;
        if (k == 0) {
          Z = make_float2(0.5f * (xk.x + xk.y), 0.5f * (xk.x - xk.y));
        } else {
          float2 E = make_float2(0.5f * (xk.x + xm.x), 0.5f * (xk.y - xm.y));
          float2 Dd = make_float2(0.5f * (xk.x - xm.x), 0.5f * (xk.y + xm.y));
          float2 O = cmulc(Dd, w);
          Z = make_float2(E.x - O.y, E.y + O.x);
        }
        z[rr][q] = Z;
      }
    }
  }
#pragma unroll
  for (int rr = 0; rr < 4; ++rr) {
    bfly4<true>(z[rr][0], z[rr][1], z[rr][2], z[rr][3]);
    float2* e = ex + (wv + 4 * rr) * 260;
#pragma unroll
    for (int m = 0; m < 4; ++m) e[lane * 4 + m] = z[rr][m];
  }
  __syncthreads();
  {
    int q12 = lane >> 2, n2 = lane & 3;
    float2 w1 = tw(128 * n2), w2 = tw(256 * n2), w3 = tw(384 * n2);
#pragma unroll
    for (int rr = 0; rr < 4; ++rr) {
      float2* e = ex + (wv + 4 * rr) * 260;
#pragma unroll
      for (int q = 0; q < 4; ++q) z[rr][q] = e[q12 * 16 + q * 4 + n2];
      z[rr][1] = cmulc(z[rr][1], w1); z[rr][2] = cmulc(z[rr][2], w2); z[rr][3] = cmulc(z[rr][3], w3);
      bfly4<true>(z[rr][0], z[rr][1], z[rr][2], z[rr][3]);
    }
    __syncthreads();
#pragma unroll
    for (int rr = 0; rr < 4; ++rr) {
      float2* e = ex + (wv + 4 * rr) * 260;
#pragma unroll
      for (int m = 0; m < 4; ++m) e[q12 * 16 + n2 + 4 * m] = z[rr][m];
    }
  }
  __syncthreads();
  {
    int q1 = lane >> 4, n1 = lane & 15;
    float2 w1 = tw(32 * n1), w2 = tw(64 * n1), w3 = tw(96 * n1);
#pragma unroll
    for (int rr = 0; rr < 4; ++rr) {
      float2* e = ex + (wv + 4 * rr) * 260;
#pragma unroll
      for (int q = 0; q < 4; ++q) z[rr][q] = e[q1 * 64 + q * 16 + n1];
      z[rr][1] = cmulc(z[rr][1], w1); z[rr][2] = cmulc(z[rr][2], w2); z[rr][3] = cmulc(z[rr][3], w3);
      bfly4<true>(z[rr][0], z[rr][1], z[rr][2], z[rr][3]);
    }
    __syncthreads();
#pragma unroll
    for (int rr = 0; rr < 4; ++rr) {
      float2* e = ex + (wv + 4 * rr) * 260;
#pragma unroll
      for (int m = 0; m < 4; ++m) e[q1 * 64 + n1 + 16 * m] = z[rr][m];
    }
  }
  __syncthreads();
  {
    float2 w1 = tw(8 * lane), w2 = tw(16 * lane), w3 = tw(24 * lane);
#pragma unroll
    for (int rr = 0; rr < 4; ++rr) {
      float2* e = ex + (wv + 4 * rr) * 260;
#pragma unroll
      for (int q = 0; q < 4; ++q) z[rr][q] = e[q * 64 + lane];
      z[rr][1] = cmulc(z[rr][1], w1); z[rr][2] = cmulc(z[rr][2], w2); z[rr][3] = cmulc(z[rr][3], w3);
      bfly4<true>(z[rr][0], z[rr][1], z[rr][2], z[rr][3]);
      const int row = b * 16 + wv + 4 * rr;
      u32* so = reinterpret_cast<u32*>(sout + row * D_);
#pragma unroll
      for (int m = 0; m < 4; ++m) {
        u32 xv = reinterpret_cast<const u32*>(xin + row * D_)[lane + 64 * m];
        u32 gv = reinterpret_cast<const u32*>(gin + row * D_)[lane + 64 * m];
        float s0 = rbf(2.0f * z[rr][m].x), s1 = rbf(2.0f * z[rr][m].y);
        float p0 = rbf(s0 * bflo(gv)), p1 = rbf(s1 * bfhi(gv));
        so[lane + 64 * m] = pack2(bflo(xv) + p0, bfhi(xv) + p1);
      }
    }
  }
}


constexpr int G1_A_BYTES = 256 * 64, G1_B_BYTES = 32 * 256, G1_STAGE = G1_A_BYTES + G1_B_BYTES, G1_NK = D_ / 32;
constexpr int G1_LDS = 3 * G1_STAGE;
#define WAIT_V(n) asm volatile("s_waitcnt vmcnt(%0)" ::"n"(n) : "memory")
#define WAIT_L(n) asm volatile("s_waitcnt lgkmcnt(%0)" ::"n"(n) : "memory")
typedef __attribute__((address_space(3))) s16x4* lds_s16x4_p;
__device__ __forceinline__ s16x4 tr_read(const char* p) {
  return __builtin_amdgcn_ds_read_tr16_b64_v4i16((lds_s16x4_p)(p));
}
__device__ __forceinline__ void gemm1_block(int tm, int tn, const u16* __restrict__ xn, const u16* __restrict__ wg,
                                            const u16* __restrict__ wu, u16* __restrict__ h, char* lds) {
  const int tid = threadIdx.x, lane = tid & 63, w = __builtin_amdgcn_readfirstlane(tid >> 6);
  const int m0 = tm * 256, f0 = tn * 64;
  const int HT = 0x1320;
  const int arow = lane >> 2, aslot = lane & 3;
  const int ach = aslot ^ ((HT >> (4 * ((arow >> 2) & 3))) & 3);
  const u16* asrc = xn + (size_t)(m0 + 64 * w + arow) * D_ + 8 * ach;
  const int bphys = lane >> 1, bhf = lane & 1;
  const int bk = bphys ^ (((bphys >> 3) & 1) << 2);
  const u16* bbase = (w < 2 ? wg : wu) + (size_t)bk * F_ + f0 + 32 * (w & 1) + 8 * bhf;
  auto issue3 = [&](int t, int buf) {
    char* st = lds + buf * G1_STAGE;
    const int k0 = t * 32;
#pragma unroll
    for (int i = 0; i < 4; ++i)
      __builtin_amdgcn_global_load_lds((const u32*)(asrc + (size_t)i * 16 * D_ + k0), (u32*)(st + (64 * w + 16 * i) * 64), 16, 0, 0);
#pragma unroll
    for (int i = 0; i < 2; ++i)
      __builtin_amdgcn_global_load_lds((const u32*)(bbase + (size_t)k0 * F_ + 16 * i), (u32*)(st + G1_A_BYTES + (2 * w + i) * 1024), 16, 0, 0);
  };
  f32x4 acc[4][8];
#pragma unroll
  for (int a = 0; a < 4; ++a)
#pragma unroll
    for (int b = 0; b < 8; ++b) acc[a][b] = f32x4{0.f, 0.f, 0.f, 0.f};
  const int fr = lane & 15, fq = lane >> 4;
  const int aoff = (64 * w + fr) * 64 + 16 * (fq ^ ((HT >> (4 * ((fr >> 2) & 3))) & 3));
  const int g = lane >> 4, q = (lane & 15) >> 2, p = lane & 3;
  const int boff0 = G1_A_BYTES + ((8 * g + 4 * (0 ^ (g & 1)) + q) * 32) + p * 8;
  const int boff1 = G1_A_BYTES + ((8 * g + 4 * (1 ^ (g & 1)) + q) * 32) + p * 8;
  #define RD_A(dst, st) _Pragma("unroll") for (int mi = 0; mi < 4; ++mi) dst[mi] = *reinterpret_cast<const bf16x8*>((st) + aoff + mi * 1024)
  #define RD_B(dst, st, n0) _Pragma("unroll") for (int ni = 0; ni < 4; ++ni) { \
      s16x4 lo = tr_read((st) + boff0 + (n0 + ni) * 1024), hi = tr_read((st) + boff1 + (n0 + ni) * 1024); \
      dst[ni] = bf16x8{lo[0], lo[1], lo[2], lo[3], hi[0], hi[1], hi[2], hi[3]}; }
  #define MMA(Bf, Af, n0) _Pragma("unroll") for (int mi = 0; mi < 4; ++mi) _Pragma("unroll") for (int ni = 0; ni < 4; ++ni) \
      acc[mi][n0 + ni] = __builtin_amdgcn_mfma_f32_16x16x32_bf16(Bf[ni], Af[mi], acc[mi][n0 + ni], 0, 0, 0)
  #define SCHED() __builtin_amdgcn_sched_barrier(0)
  bf16x8 afA[4], afB[4], bgA[4], bgB[4], bu[4];
  issue3(0, 0); issue3(1, 1); issue3(2, 2);
  WAIT_V(12);
  __builtin_amdgcn_s_barrier();
  RD_A(afA, lds); RD_B(bgA, lds, 0);
  int cur = 0;
  for (int t = 0; t < G1_NK; t += 2) {
    {
      const char* st = lds + cur * G1_STAGE;
      int nxt = cur == 2 ? 0 : cur + 1;
      const char* sn = lds + nxt * G1_STAGE;
      __builtin_amdgcn_s_waitcnt(0xC07F);
      RD_B(bu, st, 4);
      SCHED();
      __builtin_amdgcn_s_setprio(1);
      MMA(bgA, afA, 0);
      __builtin_amdgcn_s_setprio(0);
      SCHED();
      if (t + 2 < G1_NK) { WAIT_V(6); } else { WAIT_V(0); }
      WAIT_L(0);
      __builtin_amdgcn_s_barrier();
      if (t + 3 < G1_NK) issue3(t + 3, cur);
      RD_A(afB, sn); RD_B(bgB, sn, 0);
      SCHED();
      __builtin_amdgcn_s_setprio(1);
      MMA(bu, afA, 4);
      __builtin_amdgcn_s_setprio(0);
      SCHED();
      cur = nxt;
    }
    {
      const char* st = lds + cur * G1_STAGE;
      int nxt = cur == 2 ? 0 : cur + 1;
      const char* sn = lds + nxt * G1_STAGE;
      __builtin_amdgcn_s_waitcnt(0xC07F);
      RD_B(bu, st, 4);
      SCHED();
      __builtin_amdgcn_s_setprio(1);
      MMA(bgB, afB, 0);
      __builtin_amdgcn_s_setprio(0);
      SCHED();
      if (t + 3 < G1_NK) { WAIT_V(6); } else { WAIT_V(0); }
      WAIT_L(0);
      __builtin_amdgcn_s_barrier();
      if (t + 4 < G1_NK) issue3(t + 4, cur);
      if (t + 2 < G1_NK) { RD_A(afA, sn); RD_B(bgA, sn, 0); }
      SCHED();
      __builtin_amdgcn_s_setprio(1);
      MMA(bu, afB, 4);
      __builtin_amdgcn_s_setprio(0);
      SCHED();
      cur = nxt;
    }
  }
#pragma unroll
  for (int mi = 0; mi < 4; ++mi) {
    const int row = m0 + 64 * w + 16 * mi + fr;
#pragma unroll
    for (int ni = 0; ni < 4; ++ni) {
      float o[4];
#pragma unroll
      for (int j = 0; j < 4; ++j) {
        float gg = rbf(acc[mi][ni][j]), u = rbf(acc[mi][ni + 4][j]);
        float e = __builtin_amdgcn_exp2f(gg * -1.4426950408889634f);
        o[j] = rbf(gg * __builtin_amdgcn_rcpf(1.0f + e)) * u;
      }
      uint2 pk = make_uint2(pack2(o[0], o[1]), pack2(o[2], o[3]));
      *reinterpret_cast<uint2*>(h + (size_t)row * F_ + f0 + 16 * ni + 4 * fq) = pk;
    }
  }
}

constexpr int G2_A_BYTES = 64 * 128, G2_STAGE = 16384, G2_NK = F_ / 64;
constexpr int G2_LDS = 3 * G2_STAGE;
__device__ __forceinline__ int g2swz(int row) { return (row & 3) | (((row >> 3) & 1) << 2); }
__device__ __forceinline__ void gemm2_block(int tm, int tn, const u16* __restrict__ h, const u16* __restrict__ wd,
                                            u16* __restrict__ gout, char* lds) {
  const int tid = threadIdx.x, lane = tid & 63, w = __builtin_amdgcn_readfirstlane(tid >> 6);
  const int m0 = tm * 64, n0 = tn * 64, wm = w >> 1, wn = w & 1;
  const int ar = lane >> 3, as = lane & 7;
  const u16* asrc0 = h + (size_t)(m0 + 16 * w + ar) * F_ + 8 * (as ^ g2swz(ar));
  const u16* asrc1 = h + (size_t)(m0 + 16 * w + 8 + ar) * F_ + 8 * (as ^ g2swz(8 + ar));
  const int bphys = lane >> 1, bhf = lane & 1;
  const int bk = bphys ^ (((bphys >> 3) & 1) << 2);
  const u16* bsrc = wd + (size_t)bk * D_ + n0 + 16 * w + 8 * bhf;
  auto issue = [&](int t, int buf) {
    char* st = lds + buf * G2_STAGE;
    const int k0 = t * 64;
    __builtin_amdgcn_global_load_lds((const u32*)(asrc0 + k0), (u32*)(st + (16 * w) * 128), 16, 0, 0);
    __builtin_amdgcn_global_load_lds((const u32*)(asrc1 + k0), (u32*)(st + (16 * w + 8) * 128), 16, 0, 0);
    __builtin_amdgcn_global_load_lds((const u32*)(bsrc + (size_t)k0 * D_), (u32*)(st + G2_A_BYTES + w * 2048), 16, 0, 0);
    __builtin_amdgcn_global_load_lds((const u32*)(bsrc + (size_t)(k0 + 32) * D_), (u32*)(st + G2_A_BYTES + w * 2048 + 1024), 16, 0, 0);
  };
  f32x4 acc[2][2];
#pragma unroll
  for (int a = 0; a < 2; ++a)
#pragma unroll
    for (int b = 0; b < 2; ++b) acc[a][b] = f32x4{0.f, 0.f, 0.f, 0.f};
  const int fr = lane & 15, fq = lane >> 4;
  const int arow = 32 * wm + fr;
  const int aoff0 = arow * 128 + 16 * ((0 + fq) ^ g2swz(fr));
  const int aoff1 = arow * 128 + 16 * ((4 + fq) ^ g2swz(fr));
  const int g = lane >> 4, q = (lane & 15) >> 2, p = lane & 3;
  const int boff0 = G2_A_BYTES + wn * 4096 + ((8 * g + 4 * (0 ^ (g & 1)) + q) * 32) + p * 8;
  const int boff1 = G2_A_BYTES + wn * 4096 + ((8 * g + 4 * (1 ^ (g & 1)) + q) * 32) + p * 8;
  issue(0, 0); issue(1, 1);
  int cur = 0;
  for (int t = 0; t < G2_NK; ++t) {
    if (t + 1 < G2_NK) { WAIT_V(4); } else { WAIT_V(0); }
    WAIT_L(0);
    __builtin_amdgcn_s_barrier();
    int nb = cur == 0 ? 2 : cur - 1;
    if (t + 2 < G2_NK) issue(t + 2, nb);
    const char* st = lds + cur * G2_STAGE;
    bf16x8 af[2][2], bf[2][2];
#pragma unroll
    for (int mi = 0; mi < 2; ++mi) {
      af[0][mi] = *reinterpret_cast<const bf16x8*>(st + aoff0 + mi * 2048);
      af[1][mi] = *reinterpret_cast<const bf16x8*>(st + aoff1 + mi * 2048);
    }
#pragma unroll
    for (int ks = 0; ks < 2; ++ks)
#pragma unroll
      for (int j = 0; j < 2; ++j) {
        s16x4 lo = tr_read(st + boff0 + j * 2048 + ks * 1024), hi = tr_read(st + boff1 + j * 2048 + ks * 1024);
        bf[ks][j] = bf16x8{lo[0], lo[1], lo[2], lo[3], hi[0], hi[1], hi[2], hi[3]};
      }
#pragma unroll
    for (int ks = 0; ks < 2; ++ks)
#pragma unroll
      for (int mi = 0; mi < 2; ++mi)
#pragma unroll
        for (int j = 0; j < 2; ++j)
          acc[mi][j] = __builtin_amdgcn_mfma_f32_16x16x32_bf16(bf[ks][j], af[ks][mi], acc[mi][j], 0, 0, 0);
    cur = cur == 2 ? 0 : cur + 1;
  }
#pragma unroll
  for (int mi = 0; mi < 2; ++mi) {
    const int row = m0 + 32 * wm + 16 * mi + fr;
#pragma unroll
    for (int j = 0; j < 2; ++j) {
      float o[4];
#pragma unroll
      for (int e = 0; e < 4; ++e) {
        float f = rbf(acc[mi][j][e]);
        float ex = __builtin_amdgcn_exp2f(f * -1.4426950408889634f);
        o[e] = f * __builtin_amdgcn_rcpf(1.0f + ex);
      }
      uint2 pk = make_uint2(pack2(o[0], o[1]), pack2(o[2], o[3]));
      *reinterpret_cast<uint2*>(gout + (size_t)row * D_ + n0 + 32 * wn + 16 * j + 4 * fq) = pk;
    }
  }
}

__global__ __launch_bounds__(256, 2) void k1_fused(const u16* __restrict__ xn, const u16* __restrict__ wg,
                                                   const u16* __restrict__ wu, u16* __restrict__ h,
                                                   const float2* __restrict__ XT, float2* __restrict__ YT,
                                                   const u32* __restrict__ WT, const float2* __restrict__ W0h,
                                                   const float2* __restrict__ W256h) {
  __shared__ __attribute__((aligned(1024))) char lds[G1_LDS];
  const int b = blockIdx.x;
  if (b < 224) {
    int xcd = b & 7, idx = b >> 3, tm, tn;
    if (idx < 24) { tm = idx & 7; tn = xcd + 8 * (idx >> 3); }
    else { tn = 24 + (xcd >> 1); tm = 4 * (xcd & 1) + (idx - 24); }
    gemm1_block(tm, tn, xn, wg, wu, h, lds);
  } else {
    colfft_block(b - 224, XT, YT, WT, W0h, W256h, reinterpret_cast<float2*>(lds));
  }
}
__global__ __launch_bounds__(256, 2) void k2_gemm2(const u16* __restrict__ h, const u16* __restrict__ wd,
                                                   u16* __restrict__ g) {
  __shared__ __attribute__((aligned(1024))) char lds[G2_LDS];
  const int b = blockIdx.x, xcd = b & 7, idx = b >> 3;
  gemm2_block(xcd * 4 + (idx >> 3), idx & 7, h, wd, g, lds);
}
__global__ __launch_bounds__(256) void k3_final(const float2* __restrict__ YT, const u16* __restrict__ x,
                                                const u16* __restrict__ g, u16* __restrict__ out) {
  __shared__ __attribute__((aligned(16))) float2 lds[4096 + 16 * 260];
  rowifft_block(blockIdx.x, YT, out, lds, x, g);
}

extern "C" void kernel_launch(void* const* d_in, const int* in_sizes, int n_in, void* d_out, int out_size,
                              void* d_ws, size_t ws_size, hipStream_t stream) {
  const u16* x = (const u16*)d_in[0];
  const u16* wr = (const u16*)d_in[1];
  const u16* wi = (const u16*)d_in[2];
  const u16* wg = (const u16*)d_in[4];
  const u16* wu = (const u16*)d_in[5];
  const u16* wd = (const u16*)d_in[6];
  u16* out = (u16*)d_out;
  char* ws = (char*)d_ws;
  u16* xn = (u16*)(ws + 0);
  float2* XT = (float2*)(ws + (2u << 20));
  float2* YT = (float2*)(ws + (6u << 20));
  u32* WT = (u32*)(ws + (10u << 20));
  float2* W0h = (float2*)(ws + (12u << 20));
  float2* W256h = (float2*)(ws + (12u << 20) + 65536);
  u16* h = (u16*)(ws + (13u << 20));
  u16* g = (u16*)(ws + (21u << 20));
  u16* sb = (u16*)(ws + (23u << 20));

  k0_ln_rowfft<<<K0_ROWBLK + K0_WTBLK + 1, 512, 0, stream>>>(x, xn, XT, wr, wi, WT, W0h, W256h);
  k1_fused<<<224 + 256, 256, 0, stream>>>(xn, wg, wu, h, XT, YT, WT, W0h, W256h);
  k2_gemm2<<<256, 256, 0, stream>>>(h, wd, g);
  k3_final<<<128, 256, 0, stream>>>(YT, x, g, out);
}
```

```cpp
#include <hip/hip_runtime.h>
#include <hip/hip_bf16.h>
#include <stdint.h>


typedef unsigned short u16;
typedef unsigned int u32;
using bf16x8 = __attribute__((ext_vector_type(8))) short;
using s16x4 = __attribute__((ext_vector_type(4))) short;
using f32x4 = __attribute__((ext_vector_type(4))) float;

constexpr int S_ = 2048, D_ = 512, F_ = 1792, DF_ = 257;

struct TwTab { float v[4096]; };
constexpr double TW_PI = 3.14159265358979323846264338327950288;
constexpr double tw_sin(double x) {
  double x2 = x * x, term = x, sum = x;
  for (int n = 1; n < 14; ++n) { term *= -x2 / ((2 * n) * (2 * n + 1)); sum += term; }
  return sum;
}
constexpr double tw_cos(double x) {
  double x2 = x * x, term = 1.0, sum = 1.0;
  for (int n = 1; n < 14; ++n) { term *= -x2 / ((2 * n - 1) * (2 * n)); sum += term; }
  return sum;
}
constexpr TwTab make_tw() {
  TwTab t{};
  for (int j = 0; j < 2048; ++j) {
    int o = j >> 8, r = j & 255;
    double th = 2.0 * TW_PI * r / 2048.0, ph = 2.0 * TW_PI * (256 - r) / 2048.0;
    double c = 0, s = 0;
    switch (o) {
      case 0: c = tw_cos(th); s = tw_sin(th); break;
      case 1: c = tw_sin(ph); s = tw_cos(ph); break;
      case 2: c = -tw_sin(th); s = tw_cos(th); break;
      case 3: c = -tw_cos(ph); s = tw_sin(ph); break;
      case 4: c = -tw_cos(th); s = -tw_sin(th); break;
      case 5: c = -tw_sin(ph); s = -tw_cos(ph); break;
      case 6: c = tw_sin(th); s = -tw_cos(th); break;
      default: c = tw_cos(ph); s = -tw_sin(ph); break;
    }
    t.v[2 * j] = (float)c;
    t.v[2 * j + 1] = (float)(-s);
  }
  return t;
}
__device__ __attribute__((aligned(16))) constexpr TwTab TWT = make_tw();

__device__ __forceinline__ float2 tw(int j) { return reinterpret_cast<const float2*>(TWT.v)[j]; }

__device__ __forceinline__ float bf2f(u16 v) { return __uint_as_float(((u32)v) << 16); }
__device__ __forceinline__ float bflo(u32 v) { return __uint_as_float(v << 16); }
__device__ __forceinline__ float bfhi(u32 v) { return __uint_as_float(v & 0xffff0000u); }
__device__ __forceinline__ u16 f2bf(float f) { return __builtin_bit_cast(u16, (__bf16)f); }
__device__ __forceinline__ float rbf(float f) { return bf2f(f2bf(f)); }
__device__ __forceinline__ u32 pack2(float lo, float hi) { return (u32)f2bf(lo) | ((u32)f2bf(hi) << 16); }

__device__ __forceinline__ float2 cmul(float2 a, float2 b) {
  return make_float2(fmaf(a.x, b.x, -a.y * b.y), fmaf(a.x, b.y, a.y * b.x));
}
__device__ __forceinline__ float2 cmulc(float2 a, float2 b) {
  return make_float2(fmaf(a.x, b.x, a.y * b.y), fmaf(a.y, b.x, -a.x * b.y));
}
__device__ __forceinline__ float2 cadd(float2 a, float2 b) { return make_float2(a.x + b.x, a.y + b.y); }
__device__ __forceinline__ float2 csub(float2 a, float2 b) { return make_float2(a.x - b.x, a.y - b.y); }

template <bool INV>
__device__ __forceinline__ void bfly4(float2& a0, float2& a1, float2& a2, float2& a3) {
  float2 t0 = cadd(a0, a2), t1 = csub(a0, a2), t2 = cadd(a1, a3), d = csub(a1, a3);
  float2 t3 = INV ? make_float2(-d.y, d.x) : make_float2(d.y, -d.x);
  a0 = cadd(t0, t2); a1 = cadd(t1, t3); a2 = csub(t0, t2); a3 = csub(t1, t3);
}
template <bool INV>
__device__ __forceinline__ void bfly8(float2* v) {
  float2 e0 = v[0], e1 = v[2], e2 = v[4], e3 = v[6];
  float2 o0 = v[1], o1 = v[3], o2 = v[5], o3 = v[7];
  bfly4<INV>(e0, e1, e2, e3);
  bfly4<INV>(o0, o1, o2, o3);
  const float h = 0.70710678118654752440f;
  float2 p1, p2, p3;
  if (!INV) {
    p1 = make_float2((o1.x + o1.y) * h, (o1.y - o1.x) * h);
    p2 = make_float2(o2.y, -o2.x);
    p3 = make_float2((o3.y - o3.x) * h, -(o3.x + o3.y) * h);
  } else {
    p1 = make_float2((o1.x - o1.y) * h, (o1.x + o1.y) * h);
    p2 = make_float2(-o2.y, o2.x);
    p3 = make_float2(-(o3.x + o3.y) * h, (o3.x - o3.y) * h);
  }
  v[0] = cadd(e0, o0); v[4] = csub(e0, o0);
  v[1] = cadd(e1, p1); v[5] = csub(e1, p1);
  v[2] = cadd(e2, p2); v[6] = csub(e2, p2);
  v[3] = cadd(e3, p3); v[7] = csub(e3, p3);
}

__device__ __forceinline__ float wave_sum(float v) {
#define DPP_ADD(ctrl, rmask) v += __int_as_float(__builtin_amdgcn_update_dpp(0, __float_as_int(v), ctrl, rmask, 0xf, false))
  DPP_ADD(0x111, 0xf); DPP_ADD(0x112, 0xf); DPP_ADD(0x114, 0xf); DPP_ADD(0x118, 0xf);
  DPP_ADD(0x142, 0xa); DPP_ADD(0x143, 0xc);
#undef DPP_ADD
  return __int_as_float(__builtin_amdgcn_readlane(__float_as_int(v), 63));
}

#define WSYNC() do { __builtin_amdgcn_fence(__ATOMIC_RELEASE, "wavefront"); __builtin_amdgcn_s_waitcnt(0xC07F); __builtin_amdgcn_wave_barrier(); __builtin_amdgcn_fence(__ATOMIC_ACQUIRE, "wavefront"); } while (0)

__host__ __device__ __forceinline__ int wslot(int j) {
  int q1 = j & 7, q2 = (j >> 3) & 7, q3 = (j >> 6) & 7, q4 = j >> 9;
  int u = q1 * 64 + q2 * 8 + q3;
  return ((u >> 8) * 4 + q4) * 256 + (u & 255);
}

constexpr int K0_ROWBLK = 256, K0_WTBLK = 128, K0_PFBLK = 64;
__global__ __launch_bounds__(512) void k0_ln_rowfft(const u16* __restrict__ x, u16* __restrict__ xn,
                                                    float2* __restrict__ XT, const u16* __restrict__ wr,
                                                    const u16* __restrict__ wi, u32* __restrict__ WT,
                                                    float2* __restrict__ W0h, float2* __restrict__ W256h,
                                                    const u16* __restrict__ pf0, const u16* __restrict__ pf1,
                                                    const u16* __restrict__ pf2) {
  __shared__ __attribute__((aligned(16))) float2 sm[8 * 260 + 2048 + 64];
  const int tid = threadIdx.x, lane = tid & 63, wv = tid >> 6;
  const int b = blockIdx.x;
  if (b >= K0_ROWBLK) {
    int a = b - K0_ROWBLK;
    if (a < K0_WTBLK) {
      u32* tile = reinterpret_cast<u32*>(sm);
      int q1 = a & 7, q4 = (a >> 3) & 3, kq = a >> 5;
      u32 re[8], im[8];
#pragma unroll
      for (int e = 0; e < 8; ++e) {
        int idx = tid + 512 * e, i = idx >> 6, kk = idx & 63;
        int j = q1 + 512 * q4 + 8 * i;
        re[e] = wr[j * DF_ + 64 * kq + kk];
        im[e] = wi[j * DF_ + 64 * kq + kk];
      }
#pragma unroll
      for (int e = 0; e < 8; ++e) {
        int idx = tid + 512 * e, i = idx >> 6, kk = idx & 63;
        tile[i * 65 + kk] = re[e] | (im[e] << 16);
      }
      __syncthreads();
      int base = ((q1 >> 2) * 4 + q4) * 256 + (q1 & 3) * 64;
#pragma unroll
      for (int e = 0; e < 8; ++e) {
        int idx = tid + 512 * e, kk = idx >> 6, o = idx & 63;
        int q2 = o >> 3, q3 = o & 7;
        int i = q2 + 8 * q3;
        int k = 64 * kq + kk;
        if (k > 0) WT[k * 2048 + base + o] = tile[i * 65 + kk];
      }
    } else if (a >= K0_WTBLK + 4) {
      const int pb = a - K0_WTBLK - 4;
      const uint4* srcs[3] = {reinterpret_cast<const uint4*>(pf0), reinterpret_cast<const uint4*>(pf1),
                              reinterpret_cast<const uint4*>(pf2)};
      u32 acc = 0;
      const int per = (D_ * F_ * 2 / 16) / K0_PFBLK;
#pragma unroll
      for (int mth = 0; mth < 3; ++mth) {
#pragma unroll 4
        for (int e = tid; e < per; e += 512) {
          uint4 v = srcs[mth][pb * per + e];
          acc ^= v.x ^ v.y ^ v.z ^ v.w;
        }
      }
      asm volatile("" ::"v"(acc));
    } else {
      {
        int j = tid + 512 * (a - K0_WTBLK);
        int jm = (2048 - j) & 2047;
        float a0r = bf2f(wr[j * DF_]), a0i = bf2f(wi[j * DF_]);
        float b0r = bf2f(wr[jm * DF_]), b0i = bf2f(wi[jm * DF_]);
        float a1r = bf2f(wr[j * DF_ + 256]), a1i = bf2f(wi[j * DF_ + 256]);
        float b1r = bf2f(wr[jm * DF_ + 256]), b1i = bf2f(wi[jm * DF_ + 256]);
        int sl = wslot(j);
        W0h[sl] = make_float2(0.5f * (a0r + b0r), 0.5f * (a0i - b0i));
        W256h[sl] = make_float2(0.5f * (a1r + b1r), 0.5f * (a1i - b1i));
      }
    }
    return;
  }
  float2* ex = sm + wv * 260;
  float2* ot = sm + 8 * 260;
  const float2 tA1 = tw(8 * lane), tA2 = tw(16 * lane), tA3 = tw(24 * lane);
  const float2 tB1 = tw(32 * (lane & 15)), tB2 = tw(64 * (lane & 15)), tB3 = tw(96 * (lane & 15));
  const float2 tC1 = tw(128 * (lane & 3)), tC2 = tw(256 * (lane & 3)), tC3 = tw(384 * (lane & 3));
  float2 tP[4];
#pragma unroll
  for (int i = 0; i < 4; ++i) tP[i] = tw(4 * (lane + 64 * i));
  float2 z[4];
  {
    const int row = b * 8 + wv;
    const u32* xr = reinterpret_cast<const u32*>(x + row * D_);
    u32 v[4];
#pragma unroll
    for (int m = 0; m < 4; ++m) v[m] = xr[lane + 64 * m];
    float f[8];
    float sum = 0.f;
#pragma unroll
    for (int m = 0; m < 4; ++m) { f[2 * m] = bflo(v[m]); f[2 * m + 1] = bfhi(v[m]); sum += f[2 * m] + f[2 * m + 1]; }
    sum = wave_sum(sum);
    float mean = sum * (1.0f / 512.0f);
    float sq = 0.f;
#pragma unroll
    for (int e = 0; e < 8; ++e) { float d = f[e] - mean; sq = fmaf(d, d, sq); }
    sq = wave_sum(sq);
    float var = sq * (1.0f / 512.0f);
    float mb = rbf(mean), vb = rbf(var);
    float den = rbf(sqrtf(rbf(vb + 1e-5f)));
    float rc = __builtin_amdgcn_rcpf(den);
    rc = fmaf(fmaf(-den, rc, 1.0f), rc, rc);
    u32* xo = reinterpret_cast<u32*>(xn + row * D_);
#pragma unroll
    for (int m = 0; m < 4; ++m) {
      float d0 = rbf(f[2 * m] - mb), d1 = rbf(f[2 * m + 1] - mb);
      float q0 = d0 * rc, q1 = d1 * rc;
      q0 = fmaf(fmaf(-q0, den, d0), rc, q0);
      q1 = fmaf(fmaf(-q1, den, d1), rc, q1);
      u32 pk = pack2(q0, q1);
      xo[lane + 64 * m] = pk;
      z[m] = make_float2(bflo(pk), bfhi(pk));
    }
  }
  bfly4<false>(z[0], z[1], z[2], z[3]);
  z[1] = cmul(z[1], tA1); z[2] = cmul(z[2], tA2); z[3] = cmul(z[3], tA3);
#pragma unroll
  for (int q = 0; q < 4; ++q) ex[q * 64 + lane] = z[q];
  WSYNC();
  {
    int q1 = lane >> 4, n1 = lane & 15;
#pragma unroll
    for (int m = 0; m < 4; ++m) z[m] = ex[q1 * 64 + n1 + 16 * m];
    bfly4<false>(z[0], z[1], z[2], z[3]);
    z[1] = cmul(z[1], tB1); z[2] = cmul(z[2], tB2); z[3] = cmul(z[3], tB3);
    WSYNC();
#pragma unroll
    for (int q = 0; q < 4; ++q) ex[q1 * 64 + q * 16 + n1] = z[q];
  }
  WSYNC();
  {
    int q12 = lane >> 2, n2 = lane & 3;
#pragma unroll
    for (int m = 0; m < 4; ++m) z[m] = ex[q12 * 16 + n2 + 4 * m];
    bfly4<false>(z[0], z[1], z[2], z[3]);
    z[1] = cmul(z[1], tC1); z[2] = cmul(z[2], tC2); z[3] = cmul(z[3], tC3);
    WSYNC();
#pragma unroll
    for (int q = 0; q < 4; ++q) ex[q12 * 16 + q * 4 + n2] = z[q];
  }
  WSYNC();
  {
    int kb = (lane >> 4) + 4 * ((lane >> 2) & 3) + 16 * (lane & 3);
#pragma unroll
    for (int m = 0; m < 4; ++m) z[m] = ex[lane * 4 + m];
    bfly4<false>(z[0], z[1], z[2], z[3]);
    WSYNC();
#pragma unroll
    for (int q = 0; q < 4; ++q) ex[kb + 64 * q] = z[q];
  }
  WSYNC();
#pragma unroll
  for (int i = 0; i < 4; ++i) {
    int k = lane + 64 * i;
    float2 zk = ex[k], zm = ex[(256 - k) & 255];
    float2 E = make_float2(0.5f * (zk.x + zm.x), 0.5f * (zk.y - zm.y));
    float2 Dd = make_float2(zk.x - zm.x, zk.y + zm.y);
    float2 O = make_float2(0.5f * Dd.y, -0.5f * Dd.x);
    float2 X = cadd(E, cmul(O, tP[i]));
    if (k == 0) X = make_float2(zk.x + zk.y, zk.x - zk.y);
    ot[k * 8 + (wv ^ (k & 7))] = X;
  }
  __syncthreads();
  {
    int k = tid >> 1, hf = tid & 1;
    float4* dst = reinterpret_cast<float4*>(XT + (size_t)(b >> 1) * 4096 + k * 16 + (b & 1) * 8 + hf * 4);
#pragma unroll
    for (int j = 0; j < 2; ++j) {
      int r0 = hf * 4 + 2 * j;
      float2 a2 = ot[k * 8 + (r0 ^ (k & 7))], c2 = ot[k * 8 + ((r0 + 1) ^ (k & 7))];
      dst[j] = make_float4(a2.x, a2.y, c2.x, c2.y);
    }
  }
}

__device__ __forceinline__ void colfft_block(int k, const float2* __restrict__ XT, float2* __restrict__ YT,
                                             const u32* __restrict__ WT, const float2* __restrict__ W0h,
                                             const float2* __restrict__ W256h, float2* lds) {
  float2* bufA = lds;
  float2* bufB = lds + 2304;
  const int t = threadIdx.x;
  float2 v[8];
#pragma unroll
  for (int m = 0; m < 8; ++m) {
    int s = t + 256 * m;
    v[m] = XT[(size_t)(s >> 4) * 4096 + k * 16 + (s & 15)];
  }
  float2 tw1[7], tw2[7], tw3[7];
  const int q1 = t >> 5, n1 = t & 31, n2 = t & 3, q2r = (t >> 2) & 7;
#pragma unroll
  for (int q = 1; q < 8; ++q) {
    tw1[q - 1] = tw((t * q) & 2047);
    tw2[q - 1] = tw((8 * n1 * q) & 2047);
    tw3[q - 1] = tw((64 * n2 * q) & 2047);
  }
  bfly8<false>(v);
#pragma unroll
  for (int q = 1; q < 8; ++q) v[q] = cmul(v[q], tw1[q - 1]);
#pragma unroll
  for (int q = 0; q < 8; ++q) bufA[q * 256 + t] = v[q];
  __syncthreads();
#pragma unroll
  for (int m = 0; m < 8; ++m) v[m] = bufA[q1 * 256 + n1 + 32 * m];
  bfly8<false>(v);
#pragma unroll
  for (int q = 1; q < 8; ++q) v[q] = cmul(v[q], tw2[q - 1]);
#pragma unroll
  for (int q = 0; q < 8; ++q) bufB[q1 * 288 + q * 36 + n1] = v[q];
  __syncthreads();
#pragma unroll
  for (int m = 0; m < 8; ++m) v[m] = bufB[q1 * 288 + q2r * 36 + n2 + 4 * m];
  bfly8<false>(v);
#pragma unroll
  for (int q = 1; q < 8; ++q) v[q] = cmul(v[q], tw3[q - 1]);
#pragma unroll
  for (int q = 0; q < 8; ++q) bufA[(q1 * 8 + q2r) * 36 + q * 4 + n2] = v[q];
  __syncthreads();
#pragma unroll
  for (int h = 0; h < 2; ++h) {
    int u = t + 256 * h;
#pragma unroll
    for (int m = 0; m < 4; ++m) v[4 * h + m] = bufA[(u >> 3) * 36 + (u & 7) * 4 + m];
  }
  bfly4<false>(v[0], v[1], v[2], v[3]);
  bfly4<false>(v[4], v[5], v[6], v[7]);
  const float scale = 1.0f / (2048.0f * 512.0f);
  if (k != 0) {
#pragma unroll
    for (int i = 0; i < 8; ++i) {
      u32 w = WT[k * 2048 + i * 256 + t];
      float2 wf = make_float2(bflo(w) * scale, bfhi(w) * scale);
      v[i] = cmul(v[i], wf);
    }
  } else {
    int jj[8];
#pragma unroll
    for (int i = 0; i < 8; ++i) {
      int u = t + 256 * (i >> 2), q4 = i & 3;
      int a1 = u >> 6, a2 = (u >> 3) & 7, a3 = u & 7;
      jj[i] = a1 + 8 * a2 + 64 * a3 + 512 * q4;
      bufB[jj[i]] = v[i];
    }
    __syncthreads();
#pragma unroll
    for (int i = 0; i < 8; ++i) {
      float2 zk = v[i], zm = bufB[(2048 - jj[i]) & 2047];
      float2 A = make_float2(0.5f * (zk.x + zm.x), 0.5f * (zk.y - zm.y));
      float2 Dd = make_float2(zk.x - zm.x, zk.y + zm.y);
      float2 Bc = make_float2(0.5f * Dd.y, -0.5f * Dd.x);
      float2 w0 = W0h[i * 256 + t], w1 = W256h[i * 256 + t];
      float2 pa = cmul(A, w0), pb = cmul(Bc, w1);
      v[i] = make_float2((pa.x - pb.y) * scale, (pa.y + pb.x) * scale);
    }
    __syncthreads();
  }
  bfly4<true>(v[0], v[1], v[2], v[3]);
  bfly4<true>(v[4], v[5], v[6], v[7]);
#pragma unroll
  for (int h = 0; h < 2; ++h) {
    int u = t + 256 * h;
#pragma unroll
    for (int m = 0; m < 4; ++m) bufB[(u >> 3) * 36 + (u & 7) * 4 + m] = v[4 * h + m];
  }
  __syncthreads();
#pragma unroll
  for (int q = 0; q < 8; ++q) v[q] = bufB[(q1 * 8 + q2r) * 36 + q * 4 + n2];
#pragma unroll
  for (int q = 1; q < 8; ++q) v[q] = cmulc(v[q], tw3[q - 1]);
  bfly8<true>(v);
#pragma unroll
  for (int m = 0; m < 8; ++m) bufA[q1 * 288 + q2r * 36 + n2 + 4 * m] = v[m];
  __syncthreads();
#pragma unroll
  for (int q = 0; q < 8; ++q) v[q] = bufA[q1 * 288 + q * 36 + n1];
#pragma unroll
  for (int q = 1; q < 8; ++q) v[q] = cmulc(v[q], tw2[q - 1]);
  bfly8<true>(v);
#pragma unroll
  for (int m = 0; m < 8; ++m) bufB[q1 * 256 + n1 + 32 * m] = v[m];
  __syncthreads();
#pragma unroll
  for (int q = 0; q < 8; ++q) v[q] = bufB[q * 256 + t];
#pragma unroll
  for (int q = 1; q < 8; ++q) v[q] = cmulc(v[q], tw1[q - 1]);
  bfly8<true>(v);
#pragma unroll
  for (int m = 0; m < 8; ++m) {
    int s = t + 256 * m;
    YT[(size_t)(s >> 4) * 4096 + k * 16 + (s & 15)] = v[m];
  }
}


__device__ __forceinline__ void rowifft_block(int b, const float2* __restrict__ YT, u16* __restrict__ sout, float2* lds,
                                              const u16* __restrict__ xin, const u16* __restrict__ gin) {
  const int tid = threadIdx.x, lane = tid & 63, wv = tid >> 6;
  float2* it = lds;
  float2* ex = lds + 2048 + wv * 260;
  const float2 tA1 = tw(8 * lane), tA2 = tw(16 * lane), tA3 = tw(24 * lane);
  const float2 tB1 = tw(32 * (lane & 15)), tB2 = tw(64 * (lane & 15)), tB3 = tw(96 * (lane & 15));
  const float2 tC1 = tw(128 * (lane & 3)), tC2 = tw(256 * (lane & 3)), tC3 = tw(384 * (lane & 3));
  const int kb = (lane >> 4) + 4 * ((lane >> 2) & 3) + 16 * (lane & 3);
  float2 tP[4];
#pragma unroll
  for (int q = 0; q < 4; ++q) tP[q] = tw(4 * (kb + 64 * q));
  const int row = b * 8 + wv;
  u32 xg[8];
#pragma unroll
  for (int m = 0; m < 4; ++m) {
    xg[m] = reinterpret_cast<const u32*>(xin + row * D_)[lane + 64 * m];
    xg[4 + m] = reinterpret_cast<const u32*>(gin + row * D_)[lane + 64 * m];
  }
  {
    int k = tid >> 1, hf = tid & 1;
    const float4* src = reinterpret_cast<const float4*>(YT + (size_t)(b >> 1) * 4096 + k * 16 + (b & 1) * 8 + hf * 4);
    float4 d0 = src[0], d1 = src[1];
    int r0 = hf * 4;
    it[k * 8 + ((r0 + 0) ^ (k & 7))] = make_float2(d0.x, d0.y);
    it[k * 8 + ((r0 + 1) ^ (k & 7))] = make_float2(d0.z, d0.w);
    it[k * 8 + ((r0 + 2) ^ (k & 7))] = make_float2(d1.x, d1.y);
    it[k * 8 + ((r0 + 3) ^ (k & 7))] = make_float2(d1.z, d1.w);
  }
  __syncthreads();
  float2 z[4];
#pragma unroll
  for (int q = 0; q < 4; ++q) {
    int k = kb + 64 * q, km = (256 - k) & 255;
    float2 xk = it[k * 8 + (wv ^ (k & 7))], xm = it[km * 8 + (wv ^ (km & 7))];
    float2 E = make_float2(0.5f * (xk.x + xm.x), 0.5f * (xk.y - xm.y));
    float2 Dd = make_float2(0.5f * (xk.x - xm.x), 0.5f * (xk.y + xm.y));
    float2 O = cmulc(Dd, tP[q]);
    float2 Z = make_float2(E.x - O.y, E.y + O.x);
    if (k == 0) Z = make_float2(0.5f * (xk.x + xk.y), 0.5f * (xk.x - xk.y));
    z[q] = Z;
  }
  bfly4<true>(z[0], z[1], z[2], z[3]);
#pragma unroll
  for (int m = 0; m < 4; ++m) ex[lane * 4 + m] = z[m];
  WSYNC();
  {
    int q12 = lane >> 2, n2 = lane & 3;
#pragma unroll
    for (int q = 0; q < 4; ++q) z[q] = ex[q12 * 16 + q * 4 + n2];
    z[1] = cmulc(z[1], tC1); z[2] = cmulc(z[2], tC2); z[3] = cmulc(z[3], tC3);
    bfly4<true>(z[0], z[1], z[2], z[3]);
    WSYNC();
#pragma unroll
    for (int m = 0; m < 4; ++m) ex[q12 * 16 + n2 + 4 * m] = z[m];
  }
  WSYNC();
  {
    int q1 = lane >> 4, n1 = lane & 15;
#pragma unroll
    for (int q = 0; q < 4; ++q) z[q] = ex[q1 * 64 + q * 16 + n1];
    z[1] = cmulc(z[1], tB1); z[2] = cmulc(z[2], tB2); z[3] = cmulc(z[3], tB3);
    bfly4<true>(z[0], z[1], z[2], z[3]);
    WSYNC();
#pragma unroll
    for (int m = 0; m < 4; ++m) ex[q1 * 64 + n1 + 16 * m] = z[m];
  }
  WSYNC();
#pragma unroll
  for (int q = 0; q < 4; ++q) z[q] = ex[q * 64 + lane];
  z[1] = cmulc(z[1], tA1); z[2] = cmulc(z[2], tA2); z[3] = cmulc(z[3], tA3);
  bfly4<true>(z[0], z[1], z[2], z[3]);
  u32* so = reinterpret_cast<u32*>(sout + row * D_);
#pragma unroll
  for (int m = 0; m < 4; ++m) {
    u32 xv = xg[m], gv = xg[4 + m];
    float s0 = rbf(2.0f * z[m].x), s1 = rbf(2.0f * z[m].y);
    float p0 = rbf(s0 * bflo(gv)), p1 = rbf(s1 * bfhi(gv));
    so[lane + 64 * m] = pack2(bflo(xv) + p0, bfhi(xv) + p1);
  }
}

constexpr int G1_A_BYTES = 256 * 64, G1_B_BYTES = 32 * 256, G1_STAGE = G1_A_BYTES + G1_B_BYTES, G1_NK = D_ / 32;
constexpr int G1_LDS = 3 * G1_STAGE;
#define WAIT_V(n) asm volatile("s_waitcnt vmcnt(%0)" ::"n"(n) : "memory")
#define WAIT_L(n) asm volatile("s_waitcnt lgkmcnt(%0)" ::"n"(n) : "memory")
typedef __attribute__((address_space(3))) s16x4* lds_s16x4_p;
__device__ __forceinline__ s16x4 tr_read(const char* p) {
  return __builtin_amdgcn_ds_read_tr16_b64_v4i16((lds_s16x4_p)(p));
}
__device__ __forceinline__ void gemm1_block(int tm, int tn, const u16* __restrict__ xn, const u16* __restrict__ wg,
                                            const u16* __restrict__ wu, u16* __restrict__ h, char* lds) {
  const int tid = threadIdx.x, lane = tid & 63, w = __builtin_amdgcn_readfirstlane(tid >> 6);
  const int m0 = tm * 256, f0 = tn * 64;
  const int HT = 0x1320;
  const int arow = lane >> 2, aslot = lane & 3;
  const int ach = aslot ^ ((HT >> (4 * ((arow >> 2) & 3))) & 3);
  const u16* asrc = xn + (size_t)(m0 + 64 * w + arow) * D_ + 8 * ach;
  const int bphys = lane >> 1, bhf = lane & 1;
  const int bk = bphys ^ (((bphys >> 3) & 1) << 2);
  const u16* bbase = (w < 2 ? wg : wu) + (size_t)bk * F_ + f0 + 32 * (w & 1) + 8 * bhf;
  auto issue3 = [&](int t, int buf) {
    char* st = lds + buf * G1_STAGE;
    const int k0 = t * 32;
#pragma unroll
    for (int i = 0; i < 4; ++i)
      __builtin_amdgcn_global_load_lds((const u32*)(asrc + (size_t)i * 16 * D_ + k0), (u32*)(st + (64 * w + 16 * i) * 64), 16, 0, 0);
#pragma unroll
    for (int i = 0; i < 2; ++i)
      __builtin_amdgcn_global_load_lds((const u32*)(bbase + (size_t)k0 * F_ + 16 * i), (u32*)(st + G1_A_BYTES + (2 * w + i) * 1024), 16, 0, 0);
  };
  f32x4 acc[4][8];
#pragma unroll
  for (int a = 0; a < 4; ++a)
#pragma unroll
    for (int b = 0; b < 8; ++b) acc[a][b] = f32x4{0.f, 0.f, 0.f, 0.f};
  const int fr = lane & 15, fq = lane >> 4;
  const int aoff = (64 * w + fr) * 64 + 16 * (fq ^ ((HT >> (4 * ((fr >> 2) & 3))) & 3));
  const int g = lane >> 4, q = (lane & 15) >> 2, p = lane & 3;
  const int boff0 = G1_A_BYTES + ((8 * g + 4 * (0 ^ (g & 1)) + q) * 32) + p * 8;
  const int boff1 = G1_A_BYTES + ((8 * g + 4 * (1 ^ (g & 1)) + q) * 32) + p * 8;
  #define RD_A(dst, st) _Pragma("unroll") for (int mi = 0; mi < 4; ++mi) dst[mi] = *reinterpret_cast<const bf16x8*>((st) + aoff + mi * 1024)
  #define RD_B(dst, st, n0) _Pragma("unroll") for (int ni = 0; ni < 4; ++ni) { \
      s16x4 lo = tr_read((st) + boff0 + (n0 + ni) * 1024), hi = tr_read((st) + boff1 + (n0 + ni) * 1024); \
      dst[ni] = bf16x8{lo[0], lo[1], lo[2], lo[3], hi[0], hi[1], hi[2], hi[3]}; }
  #define MMA(Bf, Af, n0) _Pragma("unroll") for (int mi = 0; mi < 4; ++mi) _Pragma("unroll") for (int ni = 0; ni < 4; ++ni) \
      acc[mi][n0 + ni] = __builtin_amdgcn_mfma_f32_16x16x32_bf16(Bf[ni], Af[mi], acc[mi][n0 + ni], 0, 0, 0)
  #define SCHED() __builtin_amdgcn_sched_barrier(0)
  bf16x8 afA[4], afB[4], bgA[4], bgB[4], bu[4];
  issue3(0, 0); issue3(1, 1); issue3(2, 2);
  WAIT_V(12);
  __builtin_amdgcn_s_barrier();
  RD_A(afA, lds); RD_B(bgA, lds, 0);
  int cur = 0;
  for (int t = 0; t < G1_NK; t += 2) {
    {
      const char* st = lds + cur * G1_STAGE;
      int nxt = cur == 2 ? 0 : cur + 1;
      const char* sn = lds + nxt * G1_STAGE;
      __builtin_amdgcn_s_waitcnt(0xC07F);
      RD_B(bu, st, 4);
      SCHED();
      __builtin_amdgcn_s_setprio(1);
      MMA(bgA, afA, 0);
      __builtin_amdgcn_s_setprio(0);
      SCHED();
      if (t + 2 < G1_NK) { WAIT_V(6); } else { WAIT_V(0); }
      WAIT_L(0);
      __builtin_amdgcn_s_barrier();
      if (t + 3 < G1_NK) issue3(t + 3, cur);
      RD_A(afB, sn); RD_B(bgB, sn, 0);
      SCHED();
      __builtin_amdgcn_s_setprio(1);
      MMA(bu, afA, 4);
      __builtin_amdgcn_s_setprio(0);
      SCHED();
      cur = nxt;
    }
    {
      const char* st = lds + cur * G1_STAGE;
      int nxt = cur == 2 ? 0 : cur + 1;
      const char* sn = lds + nxt * G1_STAGE;
      __builtin_amdgcn_s_waitcnt(0xC07F);
      RD_B(bu, st, 4);
      SCHED();
      __builtin_amdgcn_s_setprio(1);
      MMA(bgB, afB, 0);
      __builtin_amdgcn_s_setprio(0);
      SCHED();
      if (t + 3 < G1_NK) { WAIT_V(6); } else { WAIT_V(0); }
      WAIT_L(0);
      __builtin_amdgcn_s_barrier();
      if (t + 4 < G1_NK) issue3(t + 4, cur);
      if (t + 2 < G1_NK) { RD_A(afA, sn); RD_B(bgA, sn, 0); }
      SCHED();
      __builtin_amdgcn_s_setprio(1);
      MMA(bu, afB, 4);
      __builtin_amdgcn_s_setprio(0);
      SCHED();
      cur = nxt;
    }
  }
#pragma unroll
  for (int mi = 0; mi < 4; ++mi) {
    const int row = m0 + 64 * w + 16 * mi + fr;
#pragma unroll
    for (int ni = 0; ni < 4; ++ni) {
      float o[4];
#pragma unroll
      for (int j = 0; j < 4; ++j) {
        float gg = rbf(acc[mi][ni][j]), u = rbf(acc[mi][ni + 4][j]);
        float e = __builtin_amdgcn_exp2f(gg * -1.4426950408889634f);
        o[j] = rbf(gg * __builtin_amdgcn_rcpf(1.0f + e)) * u;
      }
      uint2 pk = make_uint2(pack2(o[0], o[1]), pack2(o[2], o[3]));
      *reinterpret_cast<uint2*>(h + (size_t)row * F_ + f0 + 16 * ni + 4 * fq) = pk;
    }
  }
}

constexpr int G2_A_BYTES = 64 * 128, G2_STAGE = 16384, G2_NK = F_ / 64;
constexpr int G2_LDS = 3 * G2_STAGE;
__device__ __forceinline__ int g2swz(int row) { return (row & 3) | (((row >> 3) & 1) << 2); }
__device__ __forceinline__ void gemm2_block(int tm, int tn, const u16* __restrict__ h, const u16* __restrict__ wd,
                                            u16* __restrict__ gout, char* lds) {
  const int tid = threadIdx.x, lane = tid & 63, w = __builtin_amdgcn_readfirstlane(tid >> 6);
  const int m0 = tm * 64, n0 = tn * 64, wm = w >> 1, wn = w & 1;
  const int ar = lane >> 3, as = lane & 7;
  const u16* asrc0 = h + (size_t)(m0 + 16 * w + ar) * F_ + 8 * (as ^ g2swz(ar));
  const u16* asrc1 = h + (size_t)(m0 + 16 * w + 8 + ar) * F_ + 8 * (as ^ g2swz(8 + ar));
  const int bphys = lane >> 1, bhf = lane & 1;
  const int bk = bphys ^ (((bphys >> 3) & 1) << 2);
  const u16* bsrc = wd + (size_t)bk * D_ + n0 + 16 * w + 8 * bhf;
  auto issue = [&](int t, int buf) {
    char* st = lds + buf * G2_STAGE;
    const int k0 = t * 64;
    __builtin_amdgcn_global_load_lds((const u32*)(asrc0 + k0), (u32*)(st + (16 * w) * 128), 16, 0, 0);
    __builtin_amdgcn_global_load_lds((const u32*)(asrc1 + k0), (u32*)(st + (16 * w + 8) * 128), 16, 0, 0);
    __builtin_amdgcn_global_load_lds((const u32*)(bsrc + (size_t)k0 * D_), (u32*)(st + G2_A_BYTES + w * 2048), 16, 0, 0);
    __builtin_amdgcn_global_load_lds((const u32*)(bsrc + (size_t)(k0 + 32) * D_), (u32*)(st + G2_A_BYTES + w * 2048 + 1024), 16, 0, 0);
  };
  f32x4 acc[2][2];
#pragma unroll
  for (int a = 0; a < 2; ++a)
#pragma unroll
    for (int b = 0; b < 2; ++b) acc[a][b] = f32x4{0.f, 0.f, 0.f, 0.f};
  const int fr = lane & 15, fq = lane >> 4;
  const int arow = 32 * wm + fr;
  const int aoff0 = arow * 128 + 16 * ((0 + fq) ^ g2swz(fr));
  const int aoff1 = arow * 128 + 16 * ((4 + fq) ^ g2swz(fr));
  const int g = lane >> 4, q = (lane & 15) >> 2, p = lane & 3;
  const int boff0 = G2_A_BYTES + wn * 4096 + ((8 * g + 4 * (0 ^ (g & 1)) + q) * 32) + p * 8;
  const int boff1 = G2_A_BYTES + wn * 4096 + ((8 * g + 4 * (1 ^ (g & 1)) + q) * 32) + p * 8;
  issue(0, 0); issue(1, 1);
  int cur = 0;
  for (int t = 0; t < G2_NK; ++t) {
    if (t + 1 < G2_NK) { WAIT_V(4); } else { WAIT_V(0); }
    WAIT_L(0);
    __builtin_amdgcn_s_barrier();
    int nb = cur == 0 ? 2 : cur - 1;
    if (t + 2 < G2_NK) issue(t + 2, nb);
    const char* st = lds + cur * G2_STAGE;
    bf16x8 af[2][2], bf[2][2];
#pragma unroll
    for (int mi = 0; mi < 2; ++mi) {
      af[0][mi] = *reinterpret_cast<const bf16x8*>(st + aoff0 + mi * 2048);
      af[1][mi] = *reinterpret_cast<const bf16x8*>(st + aoff1 + mi * 2048);
    }
#pragma unroll
    for (int ks = 0; ks < 2; ++ks)
#pragma unroll
      for (int j = 0; j < 2; ++j) {
        s16x4 lo = tr_read(st + boff0 + j * 2048 + ks * 1024), hi = tr_read(st + boff1 + j * 2048 + ks * 1024);
        bf[ks][j] = bf16x8{lo[0], lo[1], lo[2], lo[3], hi[0], hi[1], hi[2], hi[3]};
      }
#pragma unroll
    for (int ks = 0; ks < 2; ++ks)
#pragma unroll
      for (int mi = 0; mi < 2; ++mi)
#pragma unroll
        for (int j = 0; j < 2; ++j)
          acc[mi][j] = __builtin_amdgcn_mfma_f32_16x16x32_bf16(bf[ks][j], af[ks][mi], acc[mi][j], 0, 0, 0);
    cur = cur == 2 ? 0 : cur + 1;
  }
#pragma unroll
  for (int mi = 0; mi < 2; ++mi) {
    const int row = m0 + 32 * wm + 16 * mi + fr;
#pragma unroll
    for (int j = 0; j < 2; ++j) {
      float o[4];
#pragma unroll
      for (int e = 0; e < 4; ++e) {
        float f = rbf(acc[mi][j][e]);
        float ex = __builtin_amdgcn_exp2f(f * -1.4426950408889634f);
        o[e] = f * __builtin_amdgcn_rcpf(1.0f + ex);
      }
      uint2 pk = make_uint2(pack2(o[0], o[1]), pack2(o[2], o[3]));
      *reinterpret_cast<uint2*>(gout + (size_t)row * D_ + n0 + 32 * wn + 16 * j + 4 * fq) = pk;
    }
  }
}

__global__ __launch_bounds__(256, 2) void k1_fused(const u16* __restrict__ xn, const u16* __restrict__ wg,
                                                   const u16* __restrict__ wu, u16* __restrict__ h,
                                                   const float2* __restrict__ XT, float2* __restrict__ YT,
                                                   const u32* __restrict__ WT, const float2* __restrict__ W0h,
                                                   const float2* __restrict__ W256h) {
  __shared__ __attribute__((aligned(1024))) char lds[G1_LDS];
  const int b = blockIdx.x;
  if (b < 224) {
    int xcd = b & 7, idx = b >> 3, tm, tn;
    if (idx < 24) { tm = idx & 7; tn = xcd + 8 * (idx >> 3); }
    else { tn = 24 + (xcd >> 1); tm = 4 * (xcd & 1) + (idx - 24); }
    gemm1_block(tm, tn, xn, wg, wu, h, lds);
  } else {
    colfft_block(b - 224, XT, YT, WT, W0h, W256h, reinterpret_cast<float2*>(lds));
  }
}
__global__ __launch_bounds__(256, 2) void k2_gemm2(const u16* __restrict__ h, const u16* __restrict__ wd,
                                                   u16* __restrict__ g) {
  __shared__ __attribute__((aligned(1024))) char lds[G2_LDS];
  const int b = blockIdx.x, xcd = b & 7, idx = b >> 3;
  gemm2_block(xcd * 4 + (idx >> 3), idx & 7, h, wd, g, lds);
}
__global__ __launch_bounds__(512) void k3_final(const float2* __restrict__ YT, const u16* __restrict__ x,
                                                const u16* __restrict__ g, u16* __restrict__ out) {
  __shared__ __attribute__((aligned(16))) float2 lds[2048 + 8 * 260];
  rowifft_block(blockIdx.x, YT, out, lds, x, g);
}

extern "C" void kernel_launch(void* const* d_in, const int* in_sizes, int n_in, void* d_out, int out_size,
                              void* d_ws, size_t ws_size, hipStream_t stream) {
  const u16* x = (const u16*)d_in[0];
  const u16* wr = (const u16*)d_in[1];
  const u16* wi = (const u16*)d_in[2];
  const u16* wg = (const u16*)d_in[4];
  const u16* wu = (const u16*)d_in[5];
  const u16* wd = (const u16*)d_in[6];
  u16* out = (u16*)d_out;
  char* ws = (char*)d_ws;
  u16* xn = (u16*)(ws + 0);
  float2* XT = (float2*)(ws + (2u << 20));
  float2* YT = (float2*)(ws + (6u << 20));
  u32* WT = (u32*)(ws + (10u << 20));
  float2* W0h = (float2*)(ws + (12u << 20));
  float2* W256h = (float2*)(ws + (12u << 20) + 65536);
  u16* h = (u16*)(ws + (13u << 20));
  u16* g = (u16*)(ws + (21u << 20));
  u16* sb = (u16*)(ws + (23u << 20));

  k0_ln_rowfft<<<K0_ROWBLK + K0_WTBLK + 4 + K0_PFBLK, 512, 0, stream>>>(x, xn, XT, wr, wi, WT, W0h, W256h, wg, wu, wd);
  k1_fused<<<224 + 256, 256, 0, stream>>>(xn, wg, wu, h, XT, YT, WT, W0h, W256h);
  k2_gemm2<<<256, 256, 0, stream>>>(h, wd, g);
  k3_final<<<256, 512, 0, stream>>>(YT, x, g, out);
}
```

```cpp
#include <hip/hip_runtime.h>
#include <hip/hip_bf16.h>
#include <stdint.h>


typedef unsigned short u16;
typedef unsigned int u32;
using bf16x8 = __attribute__((ext_vector_type(8))) short;
using s16x4 = __attribute__((ext_vector_type(4))) short;
using f32x4 = __attribute__((ext_vector_type(4))) float;

constexpr int S_ = 2048, D_ = 512, F_ = 1792, DF_ = 257;

struct TwTab { float v[4096]; };
constexpr double TW_PI = 3.14159265358979323846264338327950288;
constexpr double tw_sin(double x) {
  double x2 = x * x, term = x, sum = x;
  for (int n = 1; n < 14; ++n) { term *= -x2 / ((2 * n) * (2 * n + 1)); sum += term; }
  return sum;
}
constexpr double tw_cos(double x) {
  double x2 = x * x, term = 1.0, sum = 1.0;
  for (int n = 1; n < 14; ++n) { term *= -x2 / ((2 * n - 1) * (2 * n)); sum += term; }
  return sum;
}
constexpr TwTab make_tw() {
  TwTab t{};
  for (int j = 0; j < 2048; ++j) {
    int o = j >> 8, r = j & 255;
    double th = 2.0 * TW_PI * r / 2048.0, ph = 2.0 * TW_PI * (256 - r) / 2048.0;
    double c = 0, s = 0;
    switch (o) {
      case 0: c = tw_cos(th); s = tw_sin(th); break;
      case 1: c = tw_sin(ph); s = tw_cos(ph); break;
      case 2: c = -tw_sin(th); s = tw_cos(th); break;
      case 3: c = -tw_cos(ph); s = tw_sin(ph); break;
      case 4: c = -tw_cos(th); s = -tw_sin(th); break;
      case 5: c = -tw_sin(ph); s = -tw_cos(ph); break;
      case 6: c = tw_sin(th); s = -tw_cos(th); break;
      default: c = tw_cos(ph); s = -tw_sin(ph); break;
    }
    t.v[2 * j] = (float)c;
    t.v[2 * j + 1] = (float)(-s);
  }
  return t;
}
__device__ __attribute__((aligned(16))) constexpr TwTab TWT = make_tw();

__device__ __forceinline__ float2 tw(int j) { return reinterpret_cast<const float2*>(TWT.v)[j]; }

__device__ __forceinline__ float bf2f(u16 v) { return __uint_as_float(((u32)v) << 16); }
__device__ __forceinline__ float bflo(u32 v) { return __uint_as_float(v << 16); }
__device__ __forceinline__ float bfhi(u32 v) { return __uint_as_float(v & 0xffff0000u); }
__device__ __forceinline__ u16 f2bf(float f) { return __builtin_bit_cast(u16, (__bf16)f); }
__device__ __forceinline__ float rbf(float f) { return bf2f(f2bf(f)); }
__device__ __forceinline__ u32 pack2(float lo, float hi) { return (u32)f2bf(lo) | ((u32)f2bf(hi) << 16); }

__device__ __forceinline__ float2 cmul(float2 a, float2 b) {
  return make_float2(fmaf(a.x, b.x, -a.y * b.y), fmaf(a.x, b.y, a.y * b.x));
}
__device__ __forceinline__ float2 cmulc(float2 a, float2 b) {
  return make_float2(fmaf(a.x, b.x, a.y * b.y), fmaf(a.y, b.x, -a.x * b.y));
}
__device__ __forceinline__ float2 cadd(float2 a, float2 b) { return make_float2(a.x + b.x, a.y + b.y); }
__device__ __forceinline__ float2 csub(float2 a, float2 b) { return make_float2(a.x - b.x, a.y - b.y); }

template <bool INV>
__device__ __forceinline__ void bfly4(float2& a0, float2& a1, float2& a2, float2& a3) {
  float2 t0 = cadd(a0, a2), t1 = csub(a0, a2), t2 = cadd(a1, a3), d = csub(a1, a3);
  float2 t3 = INV ? make_float2(-d.y, d.x) : make_float2(d.y, -d.x);
  a0 = cadd(t0, t2); a1 = cadd(t1, t3); a2 = csub(t0, t2); a3 = csub(t1, t3);
}
template <bool INV>
__device__ __forceinline__ void bfly8(float2* v) {
  float2 e0 = v[0], e1 = v[2], e2 = v[4], e3 = v[6];
  float2 o0 = v[1], o1 = v[3], o2 = v[5], o3 = v[7];
  bfly4<INV>(e0, e1, e2, e3);
  bfly4<INV>(o0, o1, o2, o3);
  const float h = 0.70710678118654752440f;
  float2 p1, p2, p3;
  if (!INV) {
    p1 = make_float2((o1.x + o1.y) * h, (o1.y - o1.x) * h);
    p2 = make_float2(o2.y, -o2.x);
    p3 = make_float2((o3.y - o3.x) * h, -(o3.x + o3.y) * h);
  } else {
    p1 = make_float2((o1.x - o1.y) * h, (o1.x + o1.y) * h);
    p2 = make_float2(-o2.y, o2.x);
    p3 = make_float2(-(o3.x + o3.y) * h, (o3.x - o3.y) * h);
  }
  v[0] = cadd(e0, o0); v[4] = csub(e0, o0);
  v[1] = cadd(e1, p1); v[5] = csub(e1, p1);
  v[2] = cadd(e2, p2); v[6] = csub(e2, p2);
  v[3] = cadd(e3, p3); v[7] = csub(e3, p3);
}

__device__ __forceinline__ float wave_sum(float v) {
#define DPP_ADD(ctrl, rmask) v += __int_as_float(__builtin_amdgcn_update_dpp(0, __float_as_int(v), ctrl, rmask, 0xf, false))
  DPP_ADD(0x111, 0xf); DPP_ADD(0x112, 0xf); DPP_ADD(0x114, 0xf); DPP_ADD(0x118, 0xf);
  DPP_ADD(0x142, 0xa); DPP_ADD(0x143, 0xc);
#undef DPP_ADD
  return __int_as_float(__builtin_amdgcn_readlane(__float_as_int(v), 63));
}

#define WSYNC() do { __builtin_amdgcn_fence(__ATOMIC_RELEASE, "wavefront"); __builtin_amdgcn_s_waitcnt(0xC07F); __builtin_amdgcn_wave_barrier(); __builtin_amdgcn_fence(__ATOMIC_ACQUIRE, "wavefront"); } while (0)

__host__ __device__ __forceinline__ int wslot(int j) {
  int q1 = j & 7, q2 = (j >> 3) & 7, q3 = (j >> 6) & 7, q4 = j >> 9;
  int u = q1 * 64 + q2 * 8 + q3;
  return ((u >> 8) * 4 + q4) * 256 + (u & 255);
}

constexpr int K0_ROWBLK = 256, K0_WTBLK = 128, K0_PFBLK = 224, K0_SPBLK = 32;
__global__ __launch_bounds__(512) void k0_ln_rowfft(const u16* __restrict__ x, u16* __restrict__ xn,
                                                    float2* __restrict__ XT, const u16* __restrict__ wr,
                                                    const u16* __restrict__ wi, u32* __restrict__ WT,
                                                    float2* __restrict__ W0h, float2* __restrict__ W256h,
                                                    const u16* __restrict__ pf0, const u16* __restrict__ pf1,
                                                    const u16* __restrict__ pf2) {
  __shared__ __attribute__((aligned(16))) float2 sm[8 * 260 + 2048 + 64];
  const int tid = threadIdx.x, lane = tid & 63, wv = tid >> 6;
  const int b = blockIdx.x;
  if (b >= K0_ROWBLK) {
    int a = b - K0_ROWBLK;
    if (a < K0_WTBLK) {
      u32* tile = reinterpret_cast<u32*>(sm);
      int q1 = a & 7, q4 = (a >> 3) & 3, kq = a >> 5;
      u32 re[8], im[8];
#pragma unroll
      for (int e = 0; e < 8; ++e) {
        int idx = tid + 512 * e, i = idx >> 6, kk = idx & 63;
        int j = q1 + 512 * q4 + 8 * i;
        re[e] = wr[j * DF_ + 64 * kq + kk];
        im[e] = wi[j * DF_ + 64 * kq + kk];
      }
#pragma unroll
      for (int e = 0; e < 8; ++e) {
        int idx = tid + 512 * e, i = idx >> 6, kk = idx & 63;
        tile[i * 65 + kk] = re[e] | (im[e] << 16);
      }
      __syncthreads();
      int base = ((q1 >> 2) * 4 + q4) * 256 + (q1 & 3) * 64;
#pragma unroll
      for (int e = 0; e < 8; ++e) {
        int idx = tid + 512 * e, kk = idx >> 6, o = idx & 63;
        int q2 = o >> 3, q3 = o & 7;
        int i = q2 + 8 * q3;
        int k = 64 * kq + kk;
        if (k > 0) WT[k * 2048 + base + o] = tile[i * 65 + kk];
      }
    } else if (a >= K0_WTBLK + K0_SPBLK) {
      const int pb = a - K0_WTBLK - K0_SPBLK;
      const uint4* srcs[3] = {reinterpret_cast<const uint4*>(pf0), reinterpret_cast<const uint4*>(pf1),
                              reinterpret_cast<const uint4*>(pf2)};
      u32 acc = 0;
      const int per = (D_ * F_ * 2 / 16) / K0_PFBLK;
#pragma unroll
      for (int mth = 0; mth < 3; ++mth) {
#pragma unroll
        for (int e = tid; e < per; e += 512) {
          uint4 v = srcs[mth][pb * per + e];
          acc ^= v.x ^ v.y ^ v.z ^ v.w;
        }
      }
      asm volatile("" ::"v"(acc));
    } else {
      if (tid < 64) {
        int j = tid + 64 * (a - K0_WTBLK);
        int jm = (2048 - j) & 2047;
        float a0r = bf2f(wr[j * DF_]), a0i = bf2f(wi[j * DF_]);
        float b0r = bf2f(wr[jm * DF_]), b0i = bf2f(wi[jm * DF_]);
        float a1r = bf2f(wr[j * DF_ + 256]), a1i = bf2f(wi[j * DF_ + 256]);
        float b1r = bf2f(wr[jm * DF_ + 256]), b1i = bf2f(wi[jm * DF_ + 256]);
        int sl = wslot(j);
        W0h[sl] = make_float2(0.5f * (a0r + b0r), 0.5f * (a0i - b0i));
        W256h[sl] = make_float2(0.5f * (a1r + b1r), 0.5f * (a1i - b1i));
      }
    }
    return;
  }
  float2* ex = sm + wv * 260;
  float2* ot = sm + 8 * 260;
  const float2 tA1 = tw(8 * lane), tA2 = tw(16 * lane), tA3 = tw(24 * lane);
  const float2 tB1 = tw(32 * (lane & 15)), tB2 = tw(64 * (lane & 15)), tB3 = tw(96 * (lane & 15));
  const float2 tC1 = tw(128 * (lane & 3)), tC2 = tw(256 * (lane & 3)), tC3 = tw(384 * (lane & 3));
  float2 tP[4];
#pragma unroll
  for (int i = 0; i < 4; ++i) tP[i] = tw(4 * (lane + 64 * i));
  float2 z[4];
  {
    const int row = b * 8 + wv;
    const u32* xr = reinterpret_cast<const u32*>(x + row * D_);
    u32 v[4];
#pragma unroll
    for (int m = 0; m < 4; ++m) v[m] = xr[lane + 64 * m];
    float f[8];
    float sum = 0.f;
#pragma unroll
    for (int m = 0; m < 4; ++m) { f[2 * m] = bflo(v[m]); f[2 * m + 1] = bfhi(v[m]); sum += f[2 * m] + f[2 * m + 1]; }
    sum = wave_sum(sum);
    float mean = sum * (1.0f / 512.0f);
    float sq = 0.f;
#pragma unroll
    for (int e = 0; e < 8; ++e) { float d = f[e] - mean; sq = fmaf(d, d, sq); }
    sq = wave_sum(sq);
    float var = sq * (1.0f / 512.0f);
    float mb = rbf(mean), vb = rbf(var);
    float den = rbf(sqrtf(rbf(vb + 1e-5f)));
    float rc = __builtin_amdgcn_rcpf(den);
    rc = fmaf(fmaf(-den, rc, 1.0f), rc, rc);
    u32* xo = reinterpret_cast<u32*>(xn + row * D_);
#pragma unroll
    for (int m = 0; m < 4; ++m) {
      float d0 = rbf(f[2 * m] - mb), d1 = rbf(f[2 * m + 1] - mb);
      float q0 = d0 * rc, q1 = d1 * rc;
      q0 = fmaf(fmaf(-q0, den, d0), rc, q0);
      q1 = fmaf(fmaf(-q1, den, d1), rc, q1);
      u32 pk = pack2(q0, q1);
      xo[lane + 64 * m] = pk;
      z[m] = make_float2(bflo(pk), bfhi(pk));
    }
  }
  bfly4<false>(z[0], z[1], z[2], z[3]);
  z[1] = cmul(z[1], tA1); z[2] = cmul(z[2], tA2); z[3] = cmul(z[3], tA3);
#pragma unroll
  for (int q = 0; q < 4; ++q) ex[q * 64 + lane] = z[q];
  WSYNC();
  {
    int q1 = lane >> 4, n1 = lane & 15;
#pragma unroll
    for (int m = 0; m < 4; ++m) z[m] = ex[q1 * 64 + n1 + 16 * m];
    bfly4<false>(z[0], z[1], z[2], z[3]);
    z[1] = cmul(z[1], tB1); z[2] = cmul(z[2], tB2); z[3] = cmul(z[3], tB3);
    WSYNC();
#pragma unroll
    for (int q = 0; q < 4; ++q) ex[q1 * 64 + q * 16 + n1] = z[q];
  }
  WSYNC();
  {
    int q12 = lane >> 2, n2 = lane & 3;
#pragma unroll
    for (int m = 0; m < 4; ++m) z[m] = ex[q12 * 16 + n2 + 4 * m];
    bfly4<false>(z[0], z[1], z[2], z[3]);
    z[1] = cmul(z[1], tC1); z[2] = cmul(z[2], tC2); z[3] = cmul(z[3], tC3);
    WSYNC();
#pragma unroll
    for (int q = 0; q < 4; ++q) ex[q12 * 16 + q * 4 + n2] = z[q];
  }
  WSYNC();
  {
    int kb = (lane >> 4) + 4 * ((lane >> 2) & 3) + 16 * (lane & 3);
#pragma unroll
    for (int m = 0; m < 4; ++m) z[m] = ex[lane * 4 + m];
    bfly4<false>(z[0], z[1], z[2], z[3]);
    WSYNC();
#pragma unroll
    for (int q = 0; q < 4; ++q) ex[kb + 64 * q] = z[q];
  }
  WSYNC();
#pragma unroll
  for (int i = 0; i < 4; ++i) {
    int k = lane + 64 * i;
    float2 zk = ex[k], zm = ex[(256 - k) & 255];
    float2 E = make_float2(0.5f * (zk.x + zm.x), 0.5f * (zk.y - zm.y));
    float2 Dd = make_float2(zk.x - zm.x, zk.y + zm.y);
    float2 O = make_float2(0.5f * Dd.y, -0.5f * Dd.x);
    float2 X = cadd(E, cmul(O, tP[i]));
    if (k == 0) X = make_float2(zk.x + zk.y, zk.x - zk.y);
    ot[k * 8 + (wv ^ (k & 7))] = X;
  }
  __syncthreads();
  {
    int k = tid >> 1, hf = tid & 1;
    float4* dst = reinterpret_cast<float4*>(XT + (size_t)(b >> 1) * 4096 + k * 16 + (b & 1) * 8 + hf * 4);
#pragma unroll
    for (int j = 0; j < 2; ++j) {
      int r0 = hf * 4 + 2 * j;
      float2 a2 = ot[k * 8 + (r0 ^ (k & 7))], c2 = ot[k * 8 + ((r0 + 1) ^ (k & 7))];
      dst[j] = make_float4(a2.x, a2.y, c2.x, c2.y);
    }
  }
}

__device__ __forceinline__ void colfft_block(int k, const float2* __restrict__ XT, float2* __restrict__ YT,
                                             const u32* __restrict__ WT, const float2* __restrict__ W0h,
                                             const float2* __restrict__ W256h, float2* lds) {
  float2* bufA = lds;
  float2* bufB = lds + 2304;
  const int t = threadIdx.x;
  float2 v[8];
#pragma unroll
  for (int m = 0; m < 8; ++m) {
    int s = t + 256 * m;
    v[m] = XT[(size_t)(s >> 4) * 4096 + k * 16 + (s & 15)];
  }
  float2 tw1[7], tw2[7], tw3[7];
  const int q1 = t >> 5, n1 = t & 31, n2 = t & 3, q2r = (t >> 2) & 7;
#pragma unroll
  for (int q = 1; q < 8; ++q) {
    tw1[q - 1] = tw((t * q) & 2047);
    tw2[q - 1] = tw((8 * n1 * q) & 2047);
    tw3[q - 1] = tw((64 * n2 * q) & 2047);
  }
  bfly8<false>(v);
#pragma unroll
  for (int q = 1; q < 8; ++q) v[q] = cmul(v[q], tw1[q - 1]);
#pragma unroll
  for (int q = 0; q < 8; ++q) bufA[q * 256 + t] = v[q];
  __syncthreads();
#pragma unroll
  for (int m = 0; m < 8; ++m) v[m] = bufA[q1 * 256 + n1 + 32 * m];
  bfly8<false>(v);
#pragma unroll
  for (int q = 1; q < 8; ++q) v[q] = cmul(v[q], tw2[q - 1]);
#pragma unroll
  for (int q = 0; q < 8; ++q) bufB[q1 * 288 + q * 36 + n1] = v[q];
  __syncthreads();
#pragma unroll
  for (int m = 0; m < 8; ++m) v[m] = bufB[q1 * 288 + q2r * 36 + n2 + 4 * m];
  bfly8<false>(v);
#pragma unroll
  for (int q = 1; q < 8; ++q) v[q] = cmul(v[q], tw3[q - 1]);
#pragma unroll
  for (int q = 0; q < 8; ++q) bufA[(q1 * 8 + q2r) * 36 + q * 4 + n2] = v[q];
  __syncthreads();
#pragma unroll
  for (int h = 0; h < 2; ++h) {
    int u = t + 256 * h;
#pragma unroll
    for (int m = 0; m < 4; ++m) v[4 * h + m] = bufA[(u >> 3) * 36 + (u & 7) * 4 + m];
  }
  bfly4<false>(v[0], v[1], v[2], v[3]);
  bfly4<false>(v[4], v[5], v[6], v[7]);
  const float scale = 1.0f / (2048.0f * 512.0f);
  if (k != 0) {
#pragma unroll
    for (int i = 0; i < 8; ++i) {
      u32 w = WT[k * 2048 + i * 256 + t];
      float2 wf = make_float2(bflo(w) * scale, bfhi(w) * scale);
      v[i] = cmul(v[i], wf);
    }
  } else {
    int jj[8];
#pragma unroll
    for (int i = 0; i < 8; ++i) {
      int u = t + 256 * (i >> 2), q4 = i & 3;
      int a1 = u >> 6, a2 = (u >> 3) & 7, a3 = u & 7;
      jj[i] = a1 + 8 * a2 + 64 * a3 + 512 * q4;
      bufB[jj[i]] = v[i];
    }
    __syncthreads();
#pragma unroll
    for (int i = 0; i < 8; ++i) {
      float2 zk = v[i], zm = bufB[(2048 - jj[i]) & 2047];
      float2 A = make_float2(0.5f * (zk.x + zm.x), 0.5f * (zk.y - zm.y));
      float2 Dd = make_float2(zk.x - zm.x, zk.y + zm.y);
      float2 Bc = make_float2(0.5f * Dd.y, -0.5f * Dd.x);
      float2 w0 = W0h[i * 256 + t], w1 = W256h[i * 256 + t];
      float2 pa = cmul(A, w0), pb = cmul(Bc, w1);
      v[i] = make_float2((pa.x - pb.y) * scale, (pa.y + pb.x) * scale);
    }
    __syncthreads();
  }
  bfly4<true>(v[0], v[1], v[2], v[3]);
  bfly4<true>(v[4], v[5], v[6], v[7]);
#pragma unroll
  for (int h = 0; h < 2; ++h) {
    int u = t + 256 * h;
#pragma unroll
    for (int m = 0; m < 4; ++m) bufB[(u >> 3) * 36 + (u & 7) * 4 + m] = v[4 * h + m];
  }
  __syncthreads();
#pragma unroll
  for (int q = 0; q < 8; ++q) v[q] = bufB[(q1 * 8 + q2r) * 36 + q * 4 + n2];
#pragma unroll
  for (int q = 1; q < 8; ++q) v[q] = cmulc(v[q], tw3[q - 1]);
  bfly8<true>(v);
#pragma unroll
  for (int m = 0; m < 8; ++m) bufA[q1 * 288 + q2r * 36 + n2 + 4 * m] = v[m];
  __syncthreads();
#pragma unroll
  for (int q = 0; q < 8; ++q) v[q] = bufA[q1 * 288 + q * 36 + n1];
#pragma unroll
  for (int q = 1; q < 8; ++q) v[q] = cmulc(v[q], tw2[q - 1]);
  bfly8<true>(v);
#pragma unroll
  for (int m = 0; m < 8; ++m) bufB[q1 * 256 + n1 + 32 * m] = v[m];
  __syncthreads();
#pragma unroll
  for (int q = 0; q < 8; ++q) v[q] = bufB[q * 256 + t];
#pragma unroll
  for (int q = 1; q < 8; ++q) v[q] = cmulc(v[q], tw1[q - 1]);
  bfly8<true>(v);
#pragma unroll
  for (int m = 0; m < 8; ++m) {
    int s = t + 256 * m;
    YT[(size_t)(s >> 4) * 4096 + k * 16 + (s & 15)] = v[m];
  }
}


__device__ __forceinline__ void rowifft_block(int b, const float2* __restrict__ YT, u16* __restrict__ sout, float2* lds,
                                              const u16* __restrict__ xin, const u16* __restrict__ gin) {
  const int tid = threadIdx.x, lane = tid & 63, wv = tid >> 6;
  float2* it = lds;
  float2* ex = lds + 2048 + wv * 260;
  const float2 tA1 = tw(8 * lane), tA2 = tw(16 * lane), tA3 = tw(24 * lane);
  const float2 tB1 = tw(32 * (lane & 15)), tB2 = tw(64 * (lane & 15)), tB3 = tw(96 * (lane & 15));
  const float2 tC1 = tw(128 * (lane & 3)), tC2 = tw(256 * (lane & 3)), tC3 = tw(384 * (lane & 3));
  const int kb = (lane >> 4) + 4 * ((lane >> 2) & 3) + 16 * (lane & 3);
  float2 tP[4];
#pragma unroll
  for (int q = 0; q < 4; ++q) tP[q] = tw(4 * (kb + 64 * q));
  const int row = b * 8 + wv;
  u32 xg[8];
#pragma unroll
  for (int m = 0; m < 4; ++m) {
    xg[m] = reinterpret_cast<const u32*>(xin + row * D_)[lane + 64 * m];
    xg[4 + m] = reinterpret_cast<const u32*>(gin + row * D_)[lane + 64 * m];
  }
  {
    int k = tid >> 1, hf = tid & 1;
    const float4* src = reinterpret_cast<const float4*>(YT + (size_t)(b >> 1) * 4096 + k * 16 + (b & 1) * 8 + hf * 4);
    float4 d0 = src[0], d1 = src[1];
    int r0 = hf * 4;
    it[k * 8 + ((r0 + 0) ^ (k & 7))] = make_float2(d0.x, d0.y);
    it[k * 8 + ((r0 + 1) ^ (k & 7))] = make_float2(d0.z, d0.w);
    it[k * 8 + ((r0 + 2) ^ (k & 7))] = make_float2(d1.x, d1.y);
    it[k * 8 + ((r0 + 3) ^ (k & 7))] = make_float2(d1.z, d1.w);
  }
  __syncthreads();
  float2 z[4];
#pragma unroll
  for (int q = 0; q < 4; ++q) {
    int k = kb + 64 * q, km = (256 - k) & 255;
    float2 xk = it[k * 8 + (wv ^ (k & 7))], xm = it[km * 8 + (wv ^ (km & 7))];
    float2 E = make_float2(0.5f * (xk.x + xm.x), 0.5f * (xk.y - xm.y));
    float2 Dd = make_float2(0.5f * (xk.x - xm.x), 0.5f * (xk.y + xm.y));
    float2 O = cmulc(Dd, tP[q]);
    float2 Z = make_float2(E.x - O.y, E.y + O.x);
    if (k == 0) Z = make_float2(0.5f * (xk.x + xk.y), 0.5f * (xk.x - xk.y));
    z[q] = Z;
  }
  bfly4<true>(z[0], z[1], z[2], z[3]);
#pragma unroll
  for (int m = 0; m < 4; ++m) ex[lane * 4 + m] = z[m];
  WSYNC();
  {
    int q12 = lane >> 2, n2 = lane & 3;
#pragma unroll
    for (int q = 0; q < 4; ++q) z[q] = ex[q12 * 16 + q * 4 + n2];
    z[1] = cmulc(z[1], tC1); z[2] = cmulc(z[2], tC2); z[3] = cmulc(z[3], tC3);
    bfly4<true>(z[0], z[1], z[2], z[3]);
    WSYNC();
#pragma unroll
    for (int m = 0; m < 4; ++m) ex[q12 * 16 + n2 + 4 * m] = z[m];
  }
  WSYNC();
  {
    int q1 = lane >> 4, n1 = lane & 15;
#pragma unroll
    for (int q = 0; q < 4; ++q) z[q] = ex[q1 * 64 + q * 16 + n1];
    z[1] = cmulc(z[1], tB1); z[2] = cmulc(z[2], tB2); z[3] = cmulc(z[3], tB3);
    bfly4<true>(z[0], z[1], z[2], z[3]);
    WSYNC();
#pragma unroll
    for (int m = 0; m < 4; ++m) ex[q1 * 64 + n1 + 16 * m] = z[m];
  }
  WSYNC();
#pragma unroll
  for (int q = 0; q < 4; ++q) z[q] = ex[q * 64 + lane];
  z[1] = cmulc(z[1], tA1); z[2] = cmulc(z[2], tA2); z[3] = cmulc(z[3], tA3);
  bfly4<true>(z[0], z[1], z[2], z[3]);
  u32* so = reinterpret_cast<u32*>(sout + row * D_);
#pragma unroll
  for (int m = 0; m < 4; ++m) {
    u32 xv = xg[m], gv = xg[4 + m];
    float s0 = rbf(2.0f * z[m].x), s1 = rbf(2.0f * z[m].y);
    float p0 = rbf(s0 * bflo(gv)), p1 = rbf(s1 * bfhi(gv));
    so[lane + 64 * m] = pack2(bflo(xv) + p0, bfhi(xv) + p1);
  }
}

constexpr int G1_A_BYTES = 256 * 64, G1_B_BYTES = 32 * 256, G1_STAGE = G1_A_BYTES + G1_B_BYTES, G1_NK = D_ / 32;
constexpr int G1_LDS = 3 * G1_STAGE;
#define WAIT_V(n) asm volatile("s_waitcnt vmcnt(%0)" ::"n"(n) : "memory")
#define WAIT_L(n) asm volatile("s_waitcnt lgkmcnt(%0)" ::"n"(n) : "memory")
typedef __attribute__((address_space(3))) s16x4* lds_s16x4_p;
__device__ __forceinline__ s16x4 tr_read(const char* p) {
  return __builtin_amdgcn_ds_read_tr16_b64_v4i16((lds_s16x4_p)(p));
}
__device__ __forceinline__ void gemm1_block(int tm, int tn, const u16* __restrict__ xn, const u16* __restrict__ wg,
                                            const u16* __restrict__ wu, u16* __restrict__ h, char* lds) {
  const int tid = threadIdx.x, lane = tid & 63, w = __builtin_amdgcn_readfirstlane(tid >> 6);
  const int m0 = tm * 256, f0 = tn * 64;
  const int HT = 0x1320;
  const int arow = lane >> 2, aslot = lane & 3;
  const int ach = aslot ^ ((HT >> (4 * ((arow >> 2) & 3))) & 3);
  const u16* asrc = xn + (size_t)(m0 + 64 * w + arow) * D_ + 8 * ach;
  const int bphys = lane >> 1, bhf = lane & 1;
  const int bk = bphys ^ (((bphys >> 3) & 1) << 2);
  const u16* bbase = (w < 2 ? wg : wu) + (size_t)bk * F_ + f0 + 32 * (w & 1) + 8 * bhf;
  auto issue3 = [&](int t, int buf) {
    char* st = lds + buf * G1_STAGE;
    const int k0 = t * 32;
#pragma unroll
    for (int i = 0; i < 4; ++i)
      __builtin_amdgcn_global_load_lds((const u32*)(asrc + (size_t)i * 16 * D_ + k0), (u32*)(st + (64 * w + 16 * i) * 64), 16, 0, 0);
#pragma unroll
    for (int i = 0; i < 2; ++i)
      __builtin_amdgcn_global_load_lds((const u32*)(bbase + (size_t)k0 * F_ + 16 * i), (u32*)(st + G1_A_BYTES + (2 * w + i) * 1024), 16, 0, 0);
  };
  f32x4 acc[4][8];
#pragma unroll
  for (int a = 0; a < 4; ++a)
#pragma unroll
    for (int b = 0; b < 8; ++b) acc[a][b] = f32x4{0.f, 0.f, 0.f, 0.f};
  const int fr = lane & 15, fq = lane >> 4;
  const int aoff = (64 * w + fr) * 64 + 16 * (fq ^ ((HT >> (4 * ((fr >> 2) & 3))) & 3));
  const int g = lane >> 4, q = (lane & 15) >> 2, p = lane & 3;
  const int boff0 = G1_A_BYTES + ((8 * g + 4 * (0 ^ (g & 1)) + q) * 32) + p * 8;
  const int boff1 = G1_A_BYTES + ((8 * g + 4 * (1 ^ (g & 1)) + q) * 32) + p * 8;
  #define RD_A(dst, st) _Pragma("unroll") for (int mi = 0; mi < 4; ++mi) dst[mi] = *reinterpret_cast<const bf16x8*>((st) + aoff + mi * 1024)
  #define RD_B(dst, st, n0) _Pragma("unroll") for (int ni = 0; ni < 4; ++ni) { \
      s16x4 lo = tr_read((st) + boff0 + (n0 + ni) * 1024), hi = tr_read((st) + boff1 + (n0 + ni) * 1024); \
      dst[ni] = bf16x8{lo[0], lo[1], lo[2], lo[3], hi[0], hi[1], hi[2], hi[3]}; }
  #define MMA(Bf, Af, n0) _Pragma("unroll") for (int mi = 0; mi < 4; ++mi) _Pragma("unroll") for (int ni = 0; ni < 4; ++ni) \
      acc[mi][n0 + ni] = __builtin_amdgcn_mfma_f32_16x16x32_bf16(Bf[ni], Af[mi], acc[mi][n0 + ni], 0, 0, 0)
  #define SCHED() __builtin_amdgcn_sched_barrier(0)
  bf16x8 afA[4], afB[4], bgA[4], bgB[4], bu[4];
  issue3(0, 0); issue3(1, 1); issue3(2, 2);
  WAIT_V(12);
  __builtin_amdgcn_s_barrier();
  RD_A(afA, lds); RD_B(bgA, lds, 0);
  int cur = 0;
  for (int t = 0; t < G1_NK; t += 2) {
    {
      const char* st = lds + cur * G1_STAGE;
      int nxt = cur == 2 ? 0 : cur + 1;
      const char* sn = lds + nxt * G1_STAGE;
      __builtin_amdgcn_s_waitcnt(0xC07F);
      RD_B(bu, st, 4);
      SCHED();
      __builtin_amdgcn_s_setprio(1);
      MMA(bgA, afA, 0);
      __builtin_amdgcn_s_setprio(0);
      SCHED();
      if (t + 2 < G1_NK) { WAIT_V(6); } else { WAIT_V(0); }
      WAIT_L(0);
      __builtin_amdgcn_s_barrier();
      if (t + 3 < G1_NK) issue3(t + 3, cur);
      RD_A(afB, sn); RD_B(bgB, sn, 0);
      SCHED();
      __builtin_amdgcn_s_setprio(1);
      MMA(bu, afA, 4);
      __builtin_amdgcn_s_setprio(0);
      SCHED();
      cur = nxt;
    }
    {
      const char* st = lds + cur * G1_STAGE;
      int nxt = cur == 2 ? 0 : cur + 1;
      const char* sn = lds + nxt * G1_STAGE;
      __builtin_amdgcn_s_waitcnt(0xC07F);
      RD_B(bu, st, 4);
      SCHED();
      __builtin_amdgcn_s_setprio(1);
      MMA(bgB, afB, 0);
      __builtin_amdgcn_s_setprio(0);
      SCHED();
      if (t + 3 < G1_NK) { WAIT_V(6); } else { WAIT_V(0); }
      WAIT_L(0);
      __builtin_amdgcn_s_barrier();
      if (t + 4 < G1_NK) issue3(t + 4, cur);
      if (t + 2 < G1_NK) { RD_A(afA, sn); RD_B(bgA, sn, 0); }
      SCHED();
      __builtin_amdgcn_s_setprio(1);
      MMA(bu, afB, 4);
      __builtin_amdgcn_s_setprio(0);
      SCHED();
      cur = nxt;
    }
  }
#pragma unroll
  for (int mi = 0; mi < 4; ++mi) {
    const int row = m0 + 64 * w + 16 * mi + fr;
#pragma unroll
    for (int ni = 0; ni < 4; ++ni) {
      float o[4];
#pragma unroll
      for (int j = 0; j < 4; ++j) {
        float gg = rbf(acc[mi][ni][j]), u = rbf(acc[mi][ni + 4][j]);
        float e = __builtin_amdgcn_exp2f(gg * -1.4426950408889634f);
        o[j] = rbf(gg * __builtin_amdgcn_rcpf(1.0f + e)) * u;
      }
      uint2 pk = make_uint2(pack2(o[0], o[1]), pack2(o[2], o[3]));
      *reinterpret_cast<uint2*>(h + (size_t)row * F_ + f0 + 16 * ni + 4 * fq) = pk;
    }
  }
}

constexpr int G2_A_BYTES = 64 * 128, G2_STAGE = 16384, G2_NK = F_ / 64;
constexpr int G2_LDS = 3 * G2_STAGE;
__device__ __forceinline__ int g2swz(int row) { return (row & 3) | (((row >> 3) & 1) << 2); }
__device__ __forceinline__ void gemm2_block(int tm, int tn, const u16* __restrict__ h, const u16* __restrict__ wd,
                                            u16* __restrict__ gout, char* lds) {
  const int tid = threadIdx.x, lane = tid & 63, w = __builtin_amdgcn_readfirstlane(tid >> 6);
  const int m0 = tm * 64, n0 = tn * 64, wm = w >> 1, wn = w & 1;
  const int ar = lane >> 3, as = lane & 7;
  const u16* asrc0 = h + (size_t)(m0 + 16 * w + ar) * F_ + 8 * (as ^ g2swz(ar));
  const u16* asrc1 = h + (size_t)(m0 + 16 * w + 8 + ar) * F_ + 8 * (as ^ g2swz(8 + ar));
  const int bphys = lane >> 1, bhf = lane & 1;
  const int bk = bphys ^ (((bphys >> 3) & 1) << 2);
  const u16* bsrc = wd + (size_t)bk * D_ + n0 + 16 * w + 8 * bhf;
  auto issue = [&](int t, int buf) {
    char* st = lds + buf * G2_STAGE;
    const int k0 = t * 64;
    __builtin_amdgcn_global_load_lds((const u32*)(asrc0 + k0), (u32*)(st + (16 * w) * 128), 16, 0, 0);
    __builtin_amdgcn_global_load_lds((const u32*)(asrc1 + k0), (u32*)(st + (16 * w + 8) * 128), 16, 0, 0);
    __builtin_amdgcn_global_load_lds((const u32*)(bsrc + (size_t)k0 * D_), (u32*)(st + G2_A_BYTES + w * 2048), 16, 0, 0);
    __builtin_amdgcn_global_load_lds((const u32*)(bsrc + (size_t)(k0 + 32) * D_), (u32*)(st + G2_A_BYTES + w * 2048 + 1024), 16, 0, 0);
  };
  f32x4 acc[2][2];
#pragma unroll
  for (int a = 0; a < 2; ++a)
#pragma unroll
    for (int b = 0; b < 2; ++b) acc[a][b] = f32x4{0.f, 0.f, 0.f, 0.f};
  const int fr = lane & 15, fq = lane >> 4;
  const int arow = 32 * wm + fr;
  const int aoff0 = arow * 128 + 16 * ((0 + fq) ^ g2swz(fr));
  const int aoff1 = arow * 128 + 16 * ((4 + fq) ^ g2swz(fr));
  const int g = lane >> 4, q = (lane & 15) >> 2, p = lane & 3;
  const int boff0 = G2_A_BYTES + wn * 4096 + ((8 * g + 4 * (0 ^ (g & 1)) + q) * 32) + p * 8;
  const int boff1 = G2_A_BYTES + wn * 4096 + ((8 * g + 4 * (1 ^ (g & 1)) + q) * 32) + p * 8;
  issue(0, 0); issue(1, 1);
  int cur = 0;
  for (int t = 0; t < G2_NK; ++t) {
    if (t + 1 < G2_NK) { WAIT_V(4); } else { WAIT_V(0); }
    WAIT_L(0);
    __builtin_amdgcn_s_barrier();
    int nb = cur == 0 ? 2 : cur - 1;
    if (t + 2 < G2_NK) issue(t + 2, nb);
    const char* st = lds + cur * G2_STAGE;
    bf16x8 af[2][2], bf[2][2];
#pragma unroll
    for (int mi = 0; mi < 2; ++mi) {
      af[0][mi] = *reinterpret_cast<const bf16x8*>(st + aoff0 + mi * 2048);
      af[1][mi] = *reinterpret_cast<const bf16x8*>(st + aoff1 + mi * 2048);
    }
#pragma unroll
    for (int ks = 0; ks < 2; ++ks)
#pragma unroll
      for (int j = 0; j < 2; ++j) {
        s16x4 lo = tr_read(st + boff0 + j * 2048 + ks * 1024), hi = tr_read(st + boff1 + j * 2048 + ks * 1024);
        bf[ks][j] = bf16x8{lo[0], lo[1], lo[2], lo[3], hi[0], hi[1], hi[2], hi[3]};
      }
#pragma unroll
    for (int ks = 0; ks < 2; ++ks)
#pragma unroll
      for (int mi = 0; mi < 2; ++mi)
#pragma unroll
        for (int j = 0; j < 2; ++j)
          acc[mi][j] = __builtin_amdgcn_mfma_f32_16x16x32_bf16(bf[ks][j], af[ks][mi], acc[mi][j], 0, 0, 0);
    cur = cur == 2 ? 0 : cur + 1;
  }
#pragma unroll
  for (int mi = 0; mi < 2; ++mi) {
    const int row = m0 + 32 * wm + 16 * mi + fr;
#pragma unroll
    for (int j = 0; j < 2; ++j) {
      float o[4];
#pragma unroll
      for (int e = 0; e < 4; ++e) {
        float f = rbf(acc[mi][j][e]);
        float ex = __builtin_amdgcn_exp2f(f * -1.4426950408889634f);
        o[e] = f * __builtin_amdgcn_rcpf(1.0f + ex);
      }
      uint2 pk = make_uint2(pack2(o[0], o[1]), pack2(o[2], o[3]));
      *reinterpret_cast<uint2*>(gout + (size_t)row * D_ + n0 + 32 * wn + 16 * j + 4 * fq) = pk;
    }
  }
}

__global__ __launch_bounds__(256, 2) void k1_fused(const u16* __restrict__ xn, const u16* __restrict__ wg,
                                                   const u16* __restrict__ wu, u16* __restrict__ h,
                                                   const float2* __restrict__ XT, float2* __restrict__ YT,
                                                   const u32* __restrict__ WT, const float2* __restrict__ W0h,
                                                   const float2* __restrict__ W256h) {
  __shared__ __attribute__((aligned(1024))) char lds[G1_LDS];
  const int b = blockIdx.x;
  if (b < 224) {
    int xcd = b & 7, idx = b >> 3, tm, tn;
    if (idx < 24) { tm = idx & 7; tn = xcd + 8 * (idx >> 3); }
    else { tn = 24 + (xcd >> 1); tm = 4 * (xcd & 1) + (idx - 24); }
    gemm1_block(tm, tn, xn, wg, wu, h, lds);
  } else {
    colfft_block(b - 224, XT, YT, WT, W0h, W256h, reinterpret_cast<float2*>(lds));
  }
}
__global__ __launch_bounds__(256, 2) void k2_gemm2(const u16* __restrict__ h, const u16* __restrict__ wd,
                                                   u16* __restrict__ g) {
  __shared__ __attribute__((aligned(1024))) char lds[G2_LDS];
  const int b = blockIdx.x, xcd = b & 7, idx = b >> 3;
  gemm2_block(xcd * 4 + (idx >> 3), idx & 7, h, wd, g, lds);
}
__global__ __launch_bounds__(512) void k3_final(const float2* __restrict__ YT, const u16* __restrict__ x,
                                                const u16* __restrict__ g, u16* __restrict__ out) {
  __shared__ __attribute__((aligned(16))) float2 lds[2048 + 8 * 260];
  rowifft_block(blockIdx.x, YT, out, lds, x, g);
}

extern "C" void kernel_launch(void* const* d_in, const int* in_sizes, int n_in, void* d_out, int out_size,
                              void* d_ws, size_t ws_size, hipStream_t stream) {
  const u16* x = (const u16*)d_in[0];
  const u16* wr = (const u16*)d_in[1];
  const u16* wi = (const u16*)d_in[2];
  const u16* wg = (const u16*)d_in[4];
  const u16* wu = (const u16*)d_in[5];
  const u16* wd = (const u16*)d_in[6];
  u16* out = (u16*)d_out;
  char* ws = (char*)d_ws;
  u16* xn = (u16*)(ws + 0);
  float2* XT = (float2*)(ws + (2u << 20));
  float2* YT = (float2*)(ws + (6u << 20));
  u32* WT = (u32*)(ws + (10u << 20));
  float2* W0h = (float2*)(ws + (12u << 20));
  float2* W256h = (float2*)(ws + (12u << 20) + 65536);
  u16* h = (u16*)(ws + (13u << 20));
  u16* g = (u16*)(ws + (21u << 20));
  u16* sb = (u16*)(ws + (23u << 20));

  k0_ln_rowfft<<<K0_ROWBLK + K0_WTBLK + K0_SPBLK + K0_PFBLK, 512, 0, stream>>>(x, xn, XT, wr, wi, WT, W0h, W256h, wg, wu, wd);
  k1_fused<<<224 + 256, 256, 0, stream>>>(xn, wg, wu, h, XT, YT, WT, W0h, W256h);
  k2_gemm2<<<256, 256, 0, stream>>>(h, wd, g);
  k3_final<<<256, 512, 0, stream>>>(YT, x, g, out);
}
```

```cpp
#include <hip/hip_runtime.h>
#include <hip/hip_bf16.h>
#include <stdint.h>


typedef unsigned short u16;
typedef unsigned int u32;
using bf16x8 = __attribute__((ext_vector_type(8))) short;
using s16x4 = __attribute__((ext_vector_type(4))) short;
using f32x4 = __attribute__((ext_vector_type(4))) float;

constexpr int S_ = 2048, D_ = 512, F_ = 1792, DF_ = 257;

struct TwTab { float v[4096]; };
constexpr double TW_PI = 3.14159265358979323846264338327950288;
constexpr double tw_sin(double x) {
  double x2 = x * x, term = x, sum = x;
  for (int n = 1; n < 14; ++n) { term *= -x2 / ((2 * n) * (2 * n + 1)); sum += term; }
  return sum;
}
constexpr double tw_cos(double x) {
  double x2 = x * x, term = 1.0, sum = 1.0;
  for (int n = 1; n < 14; ++n) { term *= -x2 / ((2 * n - 1) * (2 * n)); sum += term; }
  return sum;
}
constexpr TwTab make_tw() {
  TwTab t{};
  for (int j = 0; j < 2048; ++j) {
    int o = j >> 8, r = j & 255;
    double th = 2.0 * TW_PI * r / 2048.0, ph = 2.0 * TW_PI * (256 - r) / 2048.0;
    double c = 0, s = 0;
    switch (o) {
      case 0: c = tw_cos(th); s = tw_sin(th); break;
      case 1: c = tw_sin(ph); s = tw_cos(ph); break;
      case 2: c = -tw_sin(th); s = tw_cos(th); break;
      case 3: c = -tw_cos(ph); s = tw_sin(ph); break;
      case 4: c = -tw_cos(th); s = -tw_sin(th); break;
      case 5: c = -tw_sin(ph); s = -tw_cos(ph); break;
      case 6: c = tw_sin(th); s = -tw_cos(th); break;
      default: c = tw_cos(ph); s = -tw_sin(ph); break;
    }
    t.v[2 * j] = (float)c;
    t.v[2 * j + 1] = (float)(-s);
  }
  return t;
}
__device__ __attribute__((aligned(16))) constexpr TwTab TWT = make_tw();

__device__ __forceinline__ float2 tw(int j) { return reinterpret_cast<const float2*>(TWT.v)[j]; }

__device__ __forceinline__ float bf2f(u16 v) { return __uint_as_float(((u32)v) << 16); }
__device__ __forceinline__ float bflo(u32 v) { return __uint_as_float(v << 16); }
__device__ __forceinline__ float bfhi(u32 v) { return __uint_as_float(v & 0xffff0000u); }
__device__ __forceinline__ u16 f2bf(float f) { return __builtin_bit_cast(u16, (__bf16)f); }
__device__ __forceinline__ float rbf(float f) { return bf2f(f2bf(f)); }
__device__ __forceinline__ u32 pack2(float lo, float hi) { return (u32)f2bf(lo) | ((u32)f2bf(hi) << 16); }

__device__ __forceinline__ float2 cmul(float2 a, float2 b) {
  return make_float2(fmaf(a.x, b.x, -a.y * b.y), fmaf(a.x, b.y, a.y * b.x));
}
__device__ __forceinline__ float2 cmulc(float2 a, float2 b) {
  return make_float2(fmaf(a.x, b.x, a.y * b.y), fmaf(a.y, b.x, -a.x * b.y));
}
__device__ __forceinline__ float2 cadd(float2 a, float2 b) { return make_float2(a.x + b.x, a.y + b.y); }
__device__ __forceinline__ float2 csub(float2 a, float2 b) { return make_float2(a.x - b.x, a.y - b.y); }

template <bool INV>
__device__ __forceinline__ void bfly4(float2& a0, float2& a1, float2& a2, float2& a3) {
  float2 t0 = cadd(a0, a2), t1 = csub(a0, a2), t2 = cadd(a1, a3), d = csub(a1, a3);
  float2 t3 = INV ? make_float2(-d.y, d.x) : make_float2(d.y, -d.x);
  a0 = cadd(t0, t2); a1 = cadd(t1, t3); a2 = csub(t0, t2); a3 = csub(t1, t3);
}
template <bool INV>
__device__ __forceinline__ void bfly8(float2* v) {
  float2 e0 = v[0], e1 = v[2], e2 = v[4], e3 = v[6];
  float2 o0 = v[1], o1 = v[3], o2 = v[5], o3 = v[7];
  bfly4<INV>(e0, e1, e2, e3);
  bfly4<INV>(o0, o1, o2, o3);
  const float h = 0.70710678118654752440f;
  float2 p1, p2, p3;
  if (!INV) {
    p1 = make_float2((o1.x + o1.y) * h, (o1.y - o1.x) * h);
    p2 = make_float2(o2.y, -o2.x);
    p3 = make_float2((o3.y - o3.x) * h, -(o3.x + o3.y) * h);
  } else {
    p1 = make_float2((o1.x - o1.y) * h, (o1.x + o1.y) * h);
    p2 = make_float2(-o2.y, o2.x);
    p3 = make_float2(-(o3.x + o3.y) * h, (o3.x - o3.y) * h);
  }
  v[0] = cadd(e0, o0); v[4] = csub(e0, o0);
  v[1] = cadd(e1, p1); v[5] = csub(e1, p1);
  v[2] = cadd(e2, p2); v[6] = csub(e2, p2);
  v[3] = cadd(e3, p3); v[7] = csub(e3, p3);
}

__device__ __forceinline__ float wave_sum(float v) {
#define DPP_ADD(ctrl, rmask) v += __int_as_float(__builtin_amdgcn_update_dpp(0, __float_as_int(v), ctrl, rmask, 0xf, false))
  DPP_ADD(0x111, 0xf); DPP_ADD(0x112, 0xf); DPP_ADD(0x114, 0xf); DPP_ADD(0x118, 0xf);
  DPP_ADD(0x142, 0xa); DPP_ADD(0x143, 0xc);
#undef DPP_ADD
  return __int_as_float(__builtin_amdgcn_readlane(__float_as_int(v), 63));
}

#define WSYNC() do { __builtin_amdgcn_fence(__ATOMIC_RELEASE, "wavefront"); __builtin_amdgcn_s_waitcnt(0xC07F); __builtin_amdgcn_wave_barrier(); __builtin_amdgcn_fence(__ATOMIC_ACQUIRE, "wavefront"); } while (0)

__host__ __device__ __forceinline__ int wslot(int j) {
  int q1 = j & 7, q2 = (j >> 3) & 7, q3 = (j >> 6) & 7, q4 = j >> 9;
  int u = q1 * 64 + q2 * 8 + q3;
  return ((u >> 8) * 4 + q4) * 256 + (u & 255);
}

constexpr int K0_ROWBLK = 256, K0_WTBLK = 128, K0_PFBLK = 224, K0_SPBLK = 32;
__global__ __launch_bounds__(512) void k0_ln_rowfft(const u16* __restrict__ x, u16* __restrict__ xn,
                                                    float2* __restrict__ XT, const u16* __restrict__ wr,
                                                    const u16* __restrict__ wi, u32* __restrict__ WT,
                                                    float2* __restrict__ W0h, float2* __restrict__ W256h,
                                                    const u16* __restrict__ pf0, const u16* __restrict__ pf1,
                                                    const u16* __restrict__ pf2) {
  __shared__ __attribute__((aligned(16))) float2 sm[8 * 260 + 2048 + 64];
  const int tid = threadIdx.x, lane = tid & 63, wv = tid >> 6;
  const int b = blockIdx.x;
  if (b >= K0_ROWBLK) {
    int a = b - K0_ROWBLK;
    if (a < K0_WTBLK) {
      u32* tile = reinterpret_cast<u32*>(sm);
      int q1 = a & 7, q4 = (a >> 3) & 3, kq = a >> 5;
      u32 re[8], im[8];
#pragma unroll
      for (int e = 0; e < 8; ++e) {
        int idx = tid + 512 * e, i = idx >> 6, kk = idx & 63;
        int j = q1 + 512 * q4 + 8 * i;
        re[e] = wr[j * DF_ + 64 * kq + kk];
        im[e] = wi[j * DF_ + 64 * kq + kk];
      }
#pragma unroll
      for (int e = 0; e < 8; ++e) {
        int idx = tid + 512 * e, i = idx >> 6, kk = idx & 63;
        tile[i * 65 + kk] = re[e] | (im[e] << 16);
      }
      __syncthreads();
      int base = ((q1 >> 2) * 4 + q4) * 256 + (q1 & 3) * 64;
#pragma unroll
      for (int e = 0; e < 8; ++e) {
        int idx = tid + 512 * e, kk = idx >> 6, o = idx & 63;
        int q2 = o >> 3, q3 = o & 7;
        int i = q2 + 8 * q3;
        int k = 64 * kq + kk;
        if (k > 0) WT[k * 2048 + base + o] = tile[i * 65 + kk];
      }
    } else if (a >= K0_WTBLK + K0_SPBLK) {
      const int pb = a - K0_WTBLK - K0_SPBLK;
      const uint4* srcs[3] = {reinterpret_cast<const uint4*>(pf0), reinterpret_cast<const uint4*>(pf1),
                              reinterpret_cast<const uint4*>(pf2)};
      u32 acc = 0;
      const int per = (D_ * F_ * 2 / 16) / K0_PFBLK;
#pragma unroll
      for (int mth = 0; mth < 3; ++mth) {
#pragma unroll
        for (int e = tid; e < per; e += 512) {
          uint4 v = srcs[mth][pb * per + e];
          acc ^= v.x ^ v.y ^ v.z ^ v.w;
        }
      }
      asm volatile("" ::"v"(acc));
    } else {
      if (tid < 64) {
        int j = tid + 64 * (a - K0_WTBLK);
        int jm = (2048 - j) & 2047;
        float a0r = bf2f(wr[j * DF_]), a0i = bf2f(wi[j * DF_]);
        float b0r = bf2f(wr[jm * DF_]), b0i = bf2f(wi[jm * DF_]);
        float a1r = bf2f(wr[j * DF_ + 256]), a1i = bf2f(wi[j * DF_ + 256]);
        float b1r = bf2f(wr[jm * DF_ + 256]), b1i = bf2f(wi[jm * DF_ + 256]);
        int sl = wslot(j);
        W0h[sl] = make_float2(0.5f * (a0r + b0r), 0.5f * (a0i - b0i));
        W256h[sl] = make_float2(0.5f * (a1r + b1r), 0.5f * (a1i - b1i));
      }
    }
    return;
  }
  float2* ex = sm + wv * 260;
  float2* ot = sm + 8 * 260;
  const float2 tA1 = tw(8 * lane), tA2 = tw(16 * lane), tA3 = tw(24 * lane);
  const float2 tB1 = tw(32 * (lane & 15)), tB2 = tw(64 * (lane & 15)), tB3 = tw(96 * (lane & 15));
  const float2 tC1 = tw(128 * (lane & 3)), tC2 = tw(256 * (lane & 3)), tC3 = tw(384 * (lane & 3));
  float2 tP[4];
#pragma unroll
  for (int i = 0; i < 4; ++i) tP[i] = tw(4 * (lane + 64 * i));
  float2 z[4];
  {
    const int row = b * 8 + wv;
    const u32* xr = reinterpret_cast<const u32*>(x + row * D_);
    u32 v[4];
#pragma unroll
    for (int m = 0; m < 4; ++m) v[m] = xr[lane + 64 * m];
    float f[8];
    float sum = 0.f;
#pragma unroll
    for (int m = 0; m < 4; ++m) { f[2 * m] = bflo(v[m]); f[2 * m + 1] = bfhi(v[m]); sum += f[2 * m] + f[2 * m + 1]; }
    sum = wave_sum(sum);
    float mean = sum * (1.0f / 512.0f);
    float sq = 0.f;
#pragma unroll
    for (int e = 0; e < 8; ++e) { float d = f[e] - mean; sq = fmaf(d, d, sq); }
    sq = wave_sum(sq);
    float var = sq * (1.0f / 512.0f);
    float mb = rbf(mean), vb = rbf(var);
    float den = rbf(sqrtf(rbf(vb + 1e-5f)));
    float rc = __builtin_amdgcn_rcpf(den);
    rc = fmaf(fmaf(-den, rc, 1.0f), rc, rc);
    u32* xo = reinterpret_cast<u32*>(xn + row * D_);
#pragma unroll
    for (int m = 0; m < 4; ++m) {
      float d0 = rbf(f[2 * m] - mb), d1 = rbf(f[2 * m + 1] - mb);
      float q0 = d0 * rc, q1 = d1 * rc;
      q0 = fmaf(fmaf(-q0, den, d0), rc, q0);
      q1 = fmaf(fmaf(-q1, den, d1), rc, q1);
      u32 pk = pack2(q0, q1);
      xo[lane + 64 * m] = pk;
      z[m] = make_float2(bflo(pk), bfhi(pk));
    }
  }
  bfly4<false>(z[0], z[1], z[2], z[3]);
  z[1] = cmul(z[1], tA1); z[2] = cmul(z[2], tA2); z[3] = cmul(z[3], tA3);
#pragma unroll
  for (int q = 0; q < 4; ++q) ex[q * 64 + lane] = z[q];
  WSYNC();
  {
    int q1 = lane >> 4, n1 = lane & 15;
#pragma unroll
    for (int m = 0; m < 4; ++m) z[m] = ex[q1 * 64 + n1 + 16 * m];
    bfly4<false>(z[0], z[1], z[2], z[3]);
    z[1] = cmul(z[1], tB1); z[2] = cmul(z[2], tB2); z[3] = cmul(z[3], tB3);
    WSYNC();
#pragma unroll
    for (int q = 0; q < 4; ++q) ex[q1 * 64 + q * 16 + n1] = z[q];
  }
  WSYNC();
  {
    int q12 = lane >> 2, n2 = lane & 3;
#pragma unroll
    for (int m = 0; m < 4; ++m) z[m] = ex[q12 * 16 + n2 + 4 * m];
    bfly4<false>(z[0], z[1], z[2], z[3]);
    z[1] = cmul(z[1], tC1); z[2] = cmul(z[2], tC2); z[3] = cmul(z[3], tC3);
    WSYNC();
#pragma unroll
    for (int q = 0; q < 4; ++q) ex[q12 * 16 + q * 4 + n2] = z[q];
  }
  WSYNC();
  {
    int kb = (lane >> 4) + 4 * ((lane >> 2) & 3) + 16 * (lane & 3);
#pragma unroll
    for (int m = 0; m < 4; ++m) z[m] = ex[lane * 4 + m];
    bfly4<false>(z[0], z[1], z[2], z[3]);
    WSYNC();
#pragma unroll
    for (int q = 0; q < 4; ++q) ex[kb + 64 * q] = z[q];
  }
  WSYNC();
#pragma unroll
  for (int i = 0; i < 4; ++i) {
    int k = lane + 64 * i;
    float2 zk = ex[k], zm = ex[(256 - k) & 255];
    float2 E = make_float2(0.5f * (zk.x + zm.x), 0.5f * (zk.y - zm.y));
    float2 Dd = make_float2(zk.x - zm.x, zk.y + zm.y);
    float2 O = make_float2(0.5f * Dd.y, -0.5f * Dd.x);
    float2 X = cadd(E, cmul(O, tP[i]));
    if (k == 0) X = make_float2(zk.x + zk.y, zk.x - zk.y);
    ot[k * 8 + (wv ^ (k & 7))] = X;
  }
  __syncthreads();
  {
    int k = tid >> 1, hf = tid & 1;
    float4* dst = reinterpret_cast<float4*>(XT + (size_t)(b >> 1) * 4096 + k * 16 + (b & 1) * 8 + hf * 4);
#pragma unroll
    for (int j = 0; j < 2; ++j) {
      int r0 = hf * 4 + 2 * j;
      float2 a2 = ot[k * 8 + (r0 ^ (k & 7))], c2 = ot[k * 8 + ((r0 + 1) ^ (k & 7))];
      dst[j] = make_float4(a2.x, a2.y, c2.x, c2.y);
    }
  }
}

__device__ __forceinline__ void colfft_block(int k, const float2* __restrict__ XT, float2* __restrict__ YT,
                                             const u32* __restrict__ WT, const float2* __restrict__ W0h,
                                             const float2* __restrict__ W256h, float2* lds) {
  float2* bufA = lds;
  float2* bufB = lds + 2304;
  const int t = threadIdx.x;
  float2 v[8];
#pragma unroll
  for (int m = 0; m < 8; ++m) {
    int s = t + 256 * m;
    v[m] = XT[(size_t)(s >> 4) * 4096 + k * 16 + (s & 15)];
  }
  float2 tw1[7], tw2[7], tw3[7];
  const int q1 = t >> 5, n1 = t & 31, n2 = t & 3, q2r = (t >> 2) & 7;
#pragma unroll
  for (int q = 1; q < 8; ++q) {
    tw1[q - 1] = tw((t * q) & 2047);
    tw2[q - 1] = tw((8 * n1 * q) & 2047);
    tw3[q - 1] = tw((64 * n2 * q) & 2047);
  }
  bfly8<false>(v);
#pragma unroll
  for (int q = 1; q < 8; ++q) v[q] = cmul(v[q], tw1[q - 1]);
#pragma unroll
  for (int q = 0; q < 8; ++q) bufA[q * 256 + t] = v[q];
  __syncthreads();
#pragma unroll
  for (int m = 0; m < 8; ++m) v[m] = bufA[q1 * 256 + n1 + 32 * m];
  bfly8<false>(v);
#pragma unroll
  for (int q = 1; q < 8; ++q) v[q] = cmul(v[q], tw2[q - 1]);
#pragma unroll
  for (int q = 0; q < 8; ++q) bufB[q1 * 288 + q * 36 + n1] = v[q];
  __syncthreads();
#pragma unroll
  for (int m = 0; m < 8; ++m) v[m] = bufB[q1 * 288 + q2r * 36 + n2 + 4 * m];
  bfly8<false>(v);
#pragma unroll
  for (int q = 1; q < 8; ++q) v[q] = cmul(v[q], tw3[q - 1]);
#pragma unroll
  for (int q = 0; q < 8; ++q) bufA[(q1 * 8 + q2r) * 36 + q * 4 + n2] = v[q];
  __syncthreads();
#pragma unroll
  for (int h = 0; h < 2; ++h) {
    int u = t + 256 * h;
#pragma unroll
    for (int m = 0; m < 4; ++m) v[4 * h + m] = bufA[(u >> 3) * 36 + (u & 7) * 4 + m];
  }
  bfly4<false>(v[0], v[1], v[2], v[3]);
  bfly4<false>(v[4], v[5], v[6], v[7]);
  const float scale = 1.0f / (2048.0f * 512.0f);
  if (k != 0) {
#pragma unroll
    for (int i = 0; i < 8; ++i) {
      u32 w = WT[k * 2048 + i * 256 + t];
      float2 wf = make_float2(bflo(w) * scale, bfhi(w) * scale);
      v[i] = cmul(v[i], wf);
    }
  } else {
    int jj[8];
#pragma unroll
    for (int i = 0; i < 8; ++i) {
      int u = t + 256 * (i >> 2), q4 = i & 3;
      int a1 = u >> 6, a2 = (u >> 3) & 7, a3 = u & 7;
      jj[i] = a1 + 8 * a2 + 64 * a3 + 512 * q4;
      bufB[jj[i]] = v[i];
    }
    __syncthreads();
#pragma unroll
    for (int i = 0; i < 8; ++i) {
      float2 zk = v[i], zm = bufB[(2048 - jj[i]) & 2047];
      float2 A = make_float2(0.5f * (zk.x + zm.x), 0.5f * (zk.y - zm.y));
      float2 Dd = make_float2(zk.x - zm.x, zk.y + zm.y);
      float2 Bc = make_float2(0.5f * Dd.y, -0.5f * Dd.x);
      float2 w0 = W0h[i * 256 + t], w1 = W256h[i * 256 + t];
      float2 pa = cmul(A, w0), pb = cmul(Bc, w1);
      v[i] = make_float2((pa.x - pb.y) * scale, (pa.y + pb.x) * scale);
    }
    __syncthreads();
  }
  bfly4<true>(v[0], v[1], v[2], v[3]);
  bfly4<true>(v[4], v[5], v[6], v[7]);
#pragma unroll
  for (int h = 0; h < 2; ++h) {
    int u = t + 256 * h;
#pragma unroll
    for (int m = 0; m < 4; ++m) bufB[(u >> 3) * 36 + (u & 7) * 4 + m] = v[4 * h + m];
  }
  __syncthreads();
#pragma unroll
  for (int q = 0; q < 8; ++q) v[q] = bufB[(q1 * 8 + q2r) * 36 + q * 4 + n2];
#pragma unroll
  for (int q = 1; q < 8; ++q) v[q] = cmulc(v[q], tw3[q - 1]);
  bfly8<true>(v);
#pragma unroll
  for (int m = 0; m < 8; ++m) bufA[q1 * 288 + q2r * 36 + n2 + 4 * m] = v[m];
  __syncthreads();
#pragma unroll
  for (int q = 0; q < 8; ++q) v[q] = bufA[q1 * 288 + q * 36 + n1];
#pragma unroll
  for (int q = 1; q < 8; ++q) v[q] = cmulc(v[q], tw2[q - 1]);
  bfly8<true>(v);
#pragma unroll
  for (int m = 0; m < 8; ++m) bufB[q1 * 256 + n1 + 32 * m] = v[m];
  __syncthreads();
#pragma unroll
  for (int q = 0; q < 8; ++q) v[q] = bufB[q * 256 + t];
#pragma unroll
  for (int q = 1; q < 8; ++q) v[q] = cmulc(v[q], tw1[q - 1]);
  bfly8<true>(v);
#pragma unroll
  for (int m = 0; m < 8; ++m) {
    int s = t + 256 * m;
    YT[(size_t)(s >> 4) * 4096 + k * 16 + (s & 15)] = v[m];
  }
}


__device__ __forceinline__ void rowifft_block(int b, const float2* __restrict__ YT, u16* __restrict__ sout, float2* lds,
                                              const u16* __restrict__ xin, const float* __restrict__ gin) {
  const int tid = threadIdx.x, lane = tid & 63, wv = tid >> 6;
  float2* it = lds;
  float2* ex = lds + 2048 + wv * 260;
  const float2 tA1 = tw(8 * lane), tA2 = tw(16 * lane), tA3 = tw(24 * lane);
  const float2 tB1 = tw(32 * (lane & 15)), tB2 = tw(64 * (lane & 15)), tB3 = tw(96 * (lane & 15));
  const float2 tC1 = tw(128 * (lane & 3)), tC2 = tw(256 * (lane & 3)), tC3 = tw(384 * (lane & 3));
  const int kb = (lane >> 4) + 4 * ((lane >> 2) & 3) + 16 * (lane & 3);
  float2 tP[4];
#pragma unroll
  for (int q = 0; q < 4; ++q) tP[q] = tw(4 * (kb + 64 * q));
  const int row = b * 8 + wv;
  u32 xg[4];
  float2 pf[4][4];
#pragma unroll
  for (int m = 0; m < 4; ++m) {
    xg[m] = reinterpret_cast<const u32*>(xin + row * D_)[lane + 64 * m];
#pragma unroll
    for (int sp = 0; sp < 4; ++sp)
      pf[m][sp] = reinterpret_cast<const float2*>(gin + (size_t)sp * (S_ * D_) + row * D_)[lane + 64 * m];
  }
  {
    int k = tid >> 1, hf = tid & 1;
    const float4* src = reinterpret_cast<const float4*>(YT + (size_t)(b >> 1) * 4096 + k * 16 + (b & 1) * 8 + hf * 4);
    float4 d0 = src[0], d1 = src[1];
    int r0 = hf * 4;
    it[k * 8 + ((r0 + 0) ^ (k & 7))] = make_float2(d0.x, d0.y);
    it[k * 8 + ((r0 + 1) ^ (k & 7))] = make_float2(d0.z, d0.w);
    it[k * 8 + ((r0 + 2) ^ (k & 7))] = make_float2(d1.x, d1.y);
    it[k * 8 + ((r0 + 3) ^ (k & 7))] = make_float2(d1.z, d1.w);
  }
  __syncthreads();
  float2 z[4];
#pragma unroll
  for (int q = 0; q < 4; ++q) {
    int k = kb + 64 * q, km = (256 - k) & 255;
    float2 xk = it[k * 8 + (wv ^ (k & 7))], xm = it[km * 8 + (wv ^ (km & 7))];
    float2 E = make_float2(0.5f * (xk.x + xm.x), 0.5f * (xk.y - xm.y));
    float2 Dd = make_float2(0.5f * (xk.x - xm.x), 0.5f * (xk.y + xm.y));
    float2 O = cmulc(Dd, tP[q]);
    float2 Z = make_float2(E.x - O.y, E.y + O.x);
    if (k == 0) Z = make_float2(0.5f * (xk.x + xk.y), 0.5f * (xk.x - xk.y));
    z[q] = Z;
  }
  bfly4<true>(z[0], z[1], z[2], z[3]);
#pragma unroll
  for (int m = 0; m < 4; ++m) ex[lane * 4 + m] = z[m];
  WSYNC();
  {
    int q12 = lane >> 2, n2 = lane & 3;
#pragma unroll
    for (int q = 0; q < 4; ++q) z[q] = ex[q12 * 16 + q * 4 + n2];
    z[1] = cmulc(z[1], tC1); z[2] = cmulc(z[2], tC2); z[3] = cmulc(z[3], tC3);
    bfly4<true>(z[0], z[1], z[2], z[3]);
    WSYNC();
#pragma unroll
    for (int m = 0; m < 4; ++m) ex[q12 * 16 + n2 + 4 * m] = z[m];
  }
  WSYNC();
  {
    int q1 = lane >> 4, n1 = lane & 15;
#pragma unroll
    for (int q = 0; q < 4; ++q) z[q] = ex[q1 * 64 + q * 16 + n1];
    z[1] = cmulc(z[1], tB1); z[2] = cmulc(z[2], tB2); z[3] = cmulc(z[3], tB3);
    bfly4<true>(z[0], z[1], z[2], z[3]);
    WSYNC();
#pragma unroll
    for (int m = 0; m < 4; ++m) ex[q1 * 64 + n1 + 16 * m] = z[m];
  }
  WSYNC();
#pragma unroll
  for (int q = 0; q < 4; ++q) z[q] = ex[q * 64 + lane];
  z[1] = cmulc(z[1], tA1); z[2] = cmulc(z[2], tA2); z[3] = cmulc(z[3], tA3);
  bfly4<true>(z[0], z[1], z[2], z[3]);
  u32* so = reinterpret_cast<u32*>(sout + row * D_);
#pragma unroll
  for (int m = 0; m < 4; ++m) {
    u32 xv = xg[m];
    float f0 = rbf((pf[m][0].x + pf[m][1].x) + (pf[m][2].x + pf[m][3].x));
    float f1 = rbf((pf[m][0].y + pf[m][1].y) + (pf[m][2].y + pf[m][3].y));
    float g0 = rbf(f0 * __builtin_amdgcn_rcpf(1.0f + __builtin_amdgcn_exp2f(f0 * -1.4426950408889634f)));
    float g1 = rbf(f1 * __builtin_amdgcn_rcpf(1.0f + __builtin_amdgcn_exp2f(f1 * -1.4426950408889634f)));
    float s0 = rbf(2.0f * z[m].x), s1 = rbf(2.0f * z[m].y);
    float p0 = rbf(s0 * g0), p1 = rbf(s1 * g1);
    so[lane + 64 * m] = pack2(bflo(xv) + p0, bfhi(xv) + p1);
  }
}

constexpr int G1_A_BYTES = 256 * 64, G1_B_BYTES = 32 * 256, G1_STAGE = G1_A_BYTES + G1_B_BYTES, G1_NK = D_ / 32;
constexpr int G1_LDS = 3 * G1_STAGE;
#define WAIT_V(n) asm volatile("s_waitcnt vmcnt(%0)" ::"n"(n) : "memory")
#define WAIT_L(n) asm volatile("s_waitcnt lgkmcnt(%0)" ::"n"(n) : "memory")
typedef __attribute__((address_space(3))) s16x4* lds_s16x4_p;
__device__ __forceinline__ s16x4 tr_read(const char* p) {
  return __builtin_amdgcn_ds_read_tr16_b64_v4i16((lds_s16x4_p)(p));
}
__device__ __forceinline__ void gemm1_block(int tm, int tn, const u16* __restrict__ xn, const u16* __restrict__ wg,
                                            const u16* __restrict__ wu, u16* __restrict__ h, char* lds) {
  const int tid = threadIdx.x, lane = tid & 63, w = __builtin_amdgcn_readfirstlane(tid >> 6);
  const int m0 = tm * 256, f0 = tn * 64;
  const int HT = 0x1320;
  const int arow = lane >> 2, aslot = lane & 3;
  const int ach = aslot ^ ((HT >> (4 * ((arow >> 2) & 3))) & 3);
  const u16* asrc = xn + (size_t)(m0 + 64 * w + arow) * D_ + 8 * ach;
  const int bphys = lane >> 1, bhf = lane & 1;
  const int bk = bphys ^ (((bphys >> 3) & 1) << 2);
  const u16* bbase = (w < 2 ? wg : wu) + (size_t)bk * F_ + f0 + 32 * (w & 1) + 8 * bhf;
  auto issue3 = [&](int t, int buf) {
    char* st = lds + buf * G1_STAGE;
    const int k0 = t * 32;
#pragma unroll
    for (int i = 0; i < 4; ++i)
      __builtin_amdgcn_global_load_lds((const u32*)(asrc + (size_t)i * 16 * D_ + k0), (u32*)(st + (64 * w + 16 * i) * 64), 16, 0, 0);
#pragma unroll
    for (int i = 0; i < 2; ++i)
      __builtin_amdgcn_global_load_lds((const u32*)(bbase + (size_t)k0 * F_ + 16 * i), (u32*)(st + G1_A_BYTES + (2 * w + i) * 1024), 16, 0, 0);
  };
  f32x4 acc[4][8];
#pragma unroll
  for (int a = 0; a < 4; ++a)
#pragma unroll
    for (int b = 0; b < 8; ++b) acc[a][b] = f32x4{0.f, 0.f, 0.f, 0.f};
  const int fr = lane & 15, fq = lane >> 4;
  const int aoff = (64 * w + fr) * 64 + 16 * (fq ^ ((HT >> (4 * ((fr >> 2) & 3))) & 3));
  const int g = lane >> 4, q = (lane & 15) >> 2, p = lane & 3;
  const int boff0 = G1_A_BYTES + ((8 * g + 4 * (0 ^ (g & 1)) + q) * 32) + p * 8;
  const int boff1 = G1_A_BYTES + ((8 * g + 4 * (1 ^ (g & 1)) + q) * 32) + p * 8;
  #define RD_A(dst, st) _Pragma("unroll") for (int mi = 0; mi < 4; ++mi) dst[mi] = *reinterpret_cast<const bf16x8*>((st) + aoff + mi * 1024)
  #define RD_B(dst, st, n0) _Pragma("unroll") for (int ni = 0; ni < 4; ++ni) { \
      s16x4 lo = tr_read((st) + boff0 + (n0 + ni) * 1024), hi = tr_read((st) + boff1 + (n0 + ni) * 1024); \
      dst[ni] = bf16x8{lo[0], lo[1], lo[2], lo[3], hi[0], hi[1], hi[2], hi[3]}; }
  #define MMA(Bf, Af, n0) _Pragma("unroll") for (int mi = 0; mi < 4; ++mi) _Pragma("unroll") for (int ni = 0; ni < 4; ++ni) \
      acc[mi][n0 + ni] = __builtin_amdgcn_mfma_f32_16x16x32_bf16(Bf[ni], Af[mi], acc[mi][n0 + ni], 0, 0, 0)
  #define SCHED() __builtin_amdgcn_sched_barrier(0)
  bf16x8 afA[4], afB[4], bgA[4], bgB[4], bu[4];
  issue3(0, 0); issue3(1, 1); issue3(2, 2);
  WAIT_V(12);
  __builtin_amdgcn_s_barrier();
  RD_A(afA, lds); RD_B(bgA, lds, 0);
  int cur = 0;
  for (int t = 0; t < G1_NK; t += 2) {
    {
      const char* st = lds + cur * G1_STAGE;
      int nxt = cur == 2 ? 0 : cur + 1;
      const char* sn = lds + nxt * G1_STAGE;
      __builtin_amdgcn_s_waitcnt(0xC07F);
      RD_B(bu, st, 4);
      SCHED();
      __builtin_amdgcn_s_setprio(1);
      MMA(bgA, afA, 0);
      __builtin_amdgcn_s_setprio(0);
      SCHED();
      if (t + 2 < G1_NK) { WAIT_V(6); } else { WAIT_V(0); }
      WAIT_L(0);
      __builtin_amdgcn_s_barrier();
      if (t + 3 < G1_NK) issue3(t + 3, cur);
      RD_A(afB, sn); RD_B(bgB, sn, 0);
      SCHED();
      __builtin_amdgcn_s_setprio(1);
      MMA(bu, afA, 4);
      __builtin_amdgcn_s_setprio(0);
      SCHED();
      cur = nxt;
    }
    {
      const char* st = lds + cur * G1_STAGE;
      int nxt = cur == 2 ? 0 : cur + 1;
      const char* sn = lds + nxt * G1_STAGE;
      __builtin_amdgcn_s_waitcnt(0xC07F);
      RD_B(bu, st, 4);
      SCHED();
      __builtin_amdgcn_s_setprio(1);
      MMA(bgB, afB, 0);
      __builtin_amdgcn_s_setprio(0);
      SCHED();
      if (t + 3 < G1_NK) { WAIT_V(6); } else { WAIT_V(0); }
      WAIT_L(0);
      __builtin_amdgcn_s_barrier();
      if (t + 4 < G1_NK) issue3(t + 4, cur);
      if (t + 2 < G1_NK) { RD_A(afA, sn); RD_B(bgA, sn, 0); }
      SCHED();
      __builtin_amdgcn_s_setprio(1);
      MMA(bu, afB, 4);
      __builtin_amdgcn_s_setprio(0);
      SCHED();
      cur = nxt;
    }
  }
#pragma unroll
  for (int mi = 0; mi < 4; ++mi) {
    const int row = m0 + 64 * w + 16 * mi + fr;
#pragma unroll
    for (int ni = 0; ni < 4; ++ni) {
      float o[4];
#pragma unroll
      for (int j = 0; j < 4; ++j) {
        float gg = rbf(acc[mi][ni][j]), u = rbf(acc[mi][ni + 4][j]);
        float e = __builtin_amdgcn_exp2f(gg * -1.4426950408889634f);
        o[j] = rbf(gg * __builtin_amdgcn_rcpf(1.0f + e)) * u;
      }
      uint2 pk = make_uint2(pack2(o[0], o[1]), pack2(o[2], o[3]));
      *reinterpret_cast<uint2*>(h + (size_t)row * F_ + f0 + 16 * ni + 4 * fq) = pk;
    }
  }
}

constexpr int G2_A_BYTES = 128 * 128, G2_STAGE = 32768, G2_NK = 7, G2_NST = 3;
constexpr int G2_LDS = G2_NST * G2_STAGE;
__device__ __forceinline__ int g2swz(int row) { return (row & 3) | (((row >> 3) & 1) << 2); }
__device__ __forceinline__ void gemm2_block(int tm, int tn, int sp, const u16* __restrict__ h, const u16* __restrict__ wd,
                                            float* __restrict__ P, char* lds) {
  const int tid = threadIdx.x, lane = tid & 63, w = __builtin_amdgcn_readfirstlane(tid >> 6);
  const int m0 = tm * 128, n0 = tn * 128, kb = sp * 448, wm = w >> 1, wn = w & 1;
  const int ar = lane >> 3, as = lane & 7;
  const u16* asrc0 = h + (size_t)(m0 + 32 * w + ar) * F_ + kb + 8 * (as ^ g2swz(ar));
  const u16* asrc1 = h + (size_t)(m0 + 32 * w + 8 + ar) * F_ + kb + 8 * (as ^ g2swz(8 + ar));
  const int br = lane >> 4, bs = lane & 15;
  const u16* bsrc = wd + (size_t)(kb + 16 * w + br) * D_ + n0;
  auto issue = [&](int t, int buf) {
    char* st = lds + buf * G2_STAGE;
    const int k0 = t * 64;
    __builtin_amdgcn_global_load_lds((const u32*)(asrc0 + k0), (u32*)(st + (32 * w) * 128), 16, 0, 0);
    __builtin_amdgcn_global_load_lds((const u32*)(asrc1 + k0), (u32*)(st + (32 * w + 8) * 128), 16, 0, 0);
    __builtin_amdgcn_global_load_lds((const u32*)(asrc0 + (size_t)16 * F_ + k0), (u32*)(st + (32 * w + 16) * 128), 16, 0, 0);
    __builtin_amdgcn_global_load_lds((const u32*)(asrc1 + (size_t)16 * F_ + k0), (u32*)(st + (32 * w + 24) * 128), 16, 0, 0);
#pragma unroll
    for (int i = 0; i < 4; ++i)
      __builtin_amdgcn_global_load_lds((const u32*)(bsrc + (size_t)(k0 + 4 * i) * D_ + 8 * (bs ^ ((br << 2) | i))),
                                       (u32*)(st + G2_A_BYTES + (16 * w + 4 * i) * 256), 16, 0, 0);
  };
  f32x4 acc[4][4];
#pragma unroll
  for (int a = 0; a < 4; ++a)
#pragma unroll
    for (int b = 0; b < 4; ++b) acc[a][b] = f32x4{0.f, 0.f, 0.f, 0.f};
  const int fr = lane & 15, fq = lane >> 4;
  const int aoff0 = (64 * wm + fr) * 128 + 16 * ((0 + fq) ^ g2swz(fr));
  const int aoff1 = (64 * wm + fr) * 128 + 16 * ((4 + fq) ^ g2swz(fr));
  const int g = lane >> 4, q = (lane & 15) >> 2, p = lane & 3;
  int boff[2][4];
#pragma unroll
  for (int t = 0; t < 2; ++t)
#pragma unroll
    for (int j = 0; j < 4; ++j)
      boff[t][j] = G2_A_BYTES + (8 * g + 4 * t + q) * 256 + 8 * (p & 1) +
                   16 * ((8 * wn + 2 * j + (p >> 1)) ^ ((q << 2) | ((2 * g + t) & 3)));
  #define G2_RD(AF, BF, st) do { \
    _Pragma("unroll") for (int mi = 0; mi < 4; ++mi) { \
      AF[0][mi] = *reinterpret_cast<const bf16x8*>((st) + aoff0 + mi * 2048); \
      AF[1][mi] = *reinterpret_cast<const bf16x8*>((st) + aoff1 + mi * 2048); } \
    _Pragma("unroll") for (int ks = 0; ks < 2; ++ks) _Pragma("unroll") for (int j = 0; j < 4; ++j) { \
        s16x4 lo = tr_read((st) + boff[0][j] + ks * 8192), hi = tr_read((st) + boff[1][j] + ks * 8192); \
        BF[ks][j] = bf16x8{lo[0], lo[1], lo[2], lo[3], hi[0], hi[1], hi[2], hi[3]}; } } while (0)
  #define G2_MMA(AF, BF) _Pragma("unroll") for (int ks = 0; ks < 2; ++ks) _Pragma("unroll") for (int mi = 0; mi < 4; ++mi) \
      _Pragma("unroll") for (int j = 0; j < 4; ++j) \
          acc[mi][j] = __builtin_amdgcn_mfma_f32_16x16x32_bf16(BF[ks][j], AF[ks][mi], acc[mi][j], 0, 0, 0)
  bf16x8 afA[2][4], bfA[2][4], afB[2][4], bfB[2][4];
  issue(0, 0); issue(1, 1); issue(2, 2);
  WAIT_V(16);
  __builtin_amdgcn_s_barrier();
  G2_RD(afA, bfA, lds);
#pragma unroll
  for (int t = 0; t < G2_NK; ++t) {
    if (t + 1 < G2_NK) {
      if (t + 2 < G2_NK) { WAIT_V(8); } else { WAIT_V(0); }
      WAIT_L(0);
      __builtin_amdgcn_s_barrier();
      if (t + 3 < G2_NK) issue(t + 3, t % 3);
      if (t & 1) G2_RD(afA, bfA, lds + ((t + 1) % 3) * G2_STAGE); else G2_RD(afB, bfB, lds + ((t + 1) % 3) * G2_STAGE);
    }
    SCHED();
    __builtin_amdgcn_s_setprio(1);
    if (t & 1) { G2_MMA(afB, bfB); } else { G2_MMA(afA, bfA); }
    __builtin_amdgcn_s_setprio(0);
    SCHED();
  }
  float* Pb = P + (size_t)sp * (S_ * D_);
#pragma unroll
  for (int mi = 0; mi < 4; ++mi) {
    const int row = m0 + 64 * wm + 16 * mi + fr;
#pragma unroll
    for (int j = 0; j < 4; ++j)
      *reinterpret_cast<f32x4*>(Pb + (size_t)row * D_ + n0 + 64 * wn + 16 * j + 4 * fq) = acc[mi][j];
  }
}

__global__ __launch_bounds__(256, 2) void k1_fused(const u16* __restrict__ xn, const u16* __restrict__ wg,
                                                   const u16* __restrict__ wu, u16* __restrict__ h,
                                                   const float2* __restrict__ XT, float2* __restrict__ YT,
                                                   const u32* __restrict__ WT, const float2* __restrict__ W0h,
                                                   const float2* __restrict__ W256h) {
  __shared__ __attribute__((aligned(1024))) char lds[G1_LDS];
  const int b = blockIdx.x;
  if (b < 224) {
    int xcd = b & 7, idx = b >> 3, tm, tn;
    if (idx < 24) { tm = idx & 7; tn = xcd + 8 * (idx >> 3); }
    else { tn = 24 + (xcd >> 1); tm = 4 * (xcd & 1) + (idx - 24); }
    gemm1_block(tm, tn, xn, wg, wu, h, lds);
  } else {
    colfft_block(b - 224, XT, YT, WT, W0h, W256h, reinterpret_cast<float2*>(lds));
  }
}
__global__ __launch_bounds__(256) void k2_gemm2(const u16* __restrict__ h, const u16* __restrict__ wd,
                                                float* __restrict__ P) {
  __shared__ __attribute__((aligned(1024))) char lds[G2_LDS];
  const int b = blockIdx.x, xcd = b & 7, idx = b >> 3;
  gemm2_block(xcd * 2 + (idx >> 4), (idx >> 2) & 3, idx & 3, h, wd, P, lds);
}
__global__ __launch_bounds__(512) void k3_final(const float2* __restrict__ YT, const u16* __restrict__ x,
                                                const float* __restrict__ g, u16* __restrict__ out) {
  __shared__ __attribute__((aligned(16))) float2 lds[2048 + 8 * 260];
  rowifft_block(blockIdx.x, YT, out, lds, x, g);
}

extern "C" void kernel_launch(void* const* d_in, const int* in_sizes, int n_in, void* d_out, int out_size,
                              void* d_ws, size_t ws_size, hipStream_t stream) {
  const u16* x = (const u16*)d_in[0];
  const u16* wr = (const u16*)d_in[1];
  const u16* wi = (const u16*)d_in[2];
  const u16* wg = (const u16*)d_in[4];
  const u16* wu = (const u16*)d_in[5];
  const u16* wd = (const u16*)d_in[6];
  u16* out = (u16*)d_out;
  char* ws = (char*)d_ws;
  u16* xn = (u16*)(ws + 0);
  float2* XT = (float2*)(ws + (2u << 20));
  float2* YT = (float2*)(ws + (6u << 20));
  u32* WT = (u32*)(ws + (10u << 20));
  float2* W0h = (float2*)(ws + (12u << 20));
  float2* W256h = (float2*)(ws + (12u << 20) + 65536);
  u16* h = (u16*)(ws + (13u << 20));
  float* g = (float*)(ws + (24u << 20));
  u16* sb = (u16*)(ws + (23u << 20));

  k0_ln_rowfft<<<K0_ROWBLK + K0_WTBLK + K0_SPBLK + K0_PFBLK, 512, 0, stream>>>(x, xn, XT, wr, wi, WT, W0h, W256h, wg, wu, wd);
  k1_fused<<<224 + 256, 256, 0, stream>>>(xn, wg, wu, h, XT, YT, WT, W0h, W256h);
  k2_gemm2<<<256, 256, 0, stream>>>(h, wd, g);
  k3_final<<<256, 512, 0, stream>>>(YT, x, g, out);
}
```

```cpp
#include <hip/hip_runtime.h>
#include <hip/hip_bf16.h>
#include <stdint.h>


typedef unsigned short u16;
typedef unsigned int u32;
using bf16x8 = __attribute__((ext_vector_type(8))) short;
using s16x4 = __attribute__((ext_vector_type(4))) short;
using f32x4 = __attribute__((ext_vector_type(4))) float;

constexpr int S_ = 2048, D_ = 512, F_ = 1792, DF_ = 257;

struct TwTab { float v[4096]; };
constexpr double TW_PI = 3.14159265358979323846264338327950288;
constexpr double tw_sin(double x) {
  double x2 = x * x, term = x, sum = x;
  for (int n = 1; n < 14; ++n) { term *= -x2 / ((2 * n) * (2 * n + 1)); sum += term; }
  return sum;
}
constexpr double tw_cos(double x) {
  double x2 = x * x, term = 1.0, sum = 1.0;
  for (int n = 1; n < 14; ++n) { term *= -x2 / ((2 * n - 1) * (2 * n)); sum += term; }
  return sum;
}
constexpr TwTab make_tw() {
  TwTab t{};
  for (int j = 0; j < 2048; ++j) {
    int o = j >> 8, r = j & 255;
    double th = 2.0 * TW_PI * r / 2048.0, ph = 2.0 * TW_PI * (256 - r) / 2048.0;
    double c = 0, s = 0;
    switch (o) {
      case 0: c = tw_cos(th); s = tw_sin(th); break;
      case 1: c = tw_sin(ph); s = tw_cos(ph); break;
      case 2: c = -tw_sin(th); s = tw_cos(th); break;
      case 3: c = -tw_cos(ph); s = tw_sin(ph); break;
      case 4: c = -tw_cos(th); s = -tw_sin(th); break;
      case 5: c = -tw_sin(ph); s = -tw_cos(ph); break;
      case 6: c = tw_sin(th); s = -tw_cos(th); break;
      default: c = tw_cos(ph); s = -tw_sin(ph); break;
    }
    t.v[2 * j] = (float)c;
    t.v[2 * j + 1] = (float)(-s);
  }
  return t;
}
__device__ __attribute__((aligned(16))) constexpr TwTab TWT = make_tw();

__device__ __forceinline__ float2 tw(int j) { return reinterpret_cast<const float2*>(TWT.v)[j]; }

__device__ __forceinline__ float bf2f(u16 v) { return __uint_as_float(((u32)v) << 16); }
__device__ __forceinline__ float bflo(u32 v) { return __uint_as_float(v << 16); }
__device__ __forceinline__ float bfhi(u32 v) { return __uint_as_float(v & 0xffff0000u); }
__device__ __forceinline__ u16 f2bf(float f) { return __builtin_bit_cast(u16, (__bf16)f); }
__device__ __forceinline__ float rbf(float f) { return bf2f(f2bf(f)); }
__device__ __forceinline__ u32 pack2(float lo, float hi) { return (u32)f2bf(lo) | ((u32)f2bf(hi) << 16); }

__device__ __forceinline__ float2 cmul(float2 a, float2 b) {
  return make_float2(fmaf(a.x, b.x, -a.y * b.y), fmaf(a.x, b.y, a.y * b.x));
}
__device__ __forceinline__ float2 cmulc(float2 a, float2 b) {
  return make_float2(fmaf(a.x, b.x, a.y * b.y), fmaf(a.y, b.x, -a.x * b.y));
}
__device__ __forceinline__ float2 cadd(float2 a, float2 b) { return make_float2(a.x + b.x, a.y + b.y); }
__device__ __forceinline__ float2 csub(float2 a, float2 b) { return make_float2(a.x - b.x, a.y - b.y); }

template <bool INV>
__device__ __forceinline__ void bfly4(float2& a0, float2& a1, float2& a2, float2& a3) {
  float2 t0 = cadd(a0, a2), t1 = csub(a0, a2), t2 = cadd(a1, a3), d = csub(a1, a3);
  float2 t3 = INV ? make_float2(-d.y, d.x) : make_float2(d.y, -d.x);
  a0 = cadd(t0, t2); a1 = cadd(t1, t3); a2 = csub(t0, t2); a3 = csub(t1, t3);
}
template <bool INV>
__device__ __forceinline__ void bfly8(float2* v) {
  float2 e0 = v[0], e1 = v[2], e2 = v[4], e3 = v[6];
  float2 o0 = v[1], o1 = v[3], o2 = v[5], o3 = v[7];
  bfly4<INV>(e0, e1, e2, e3);
  bfly4<INV>(o0, o1, o2, o3);
  const float h = 0.70710678118654752440f;
  float2 p1, p2, p3;
  if (!INV) {
    p1 = make_float2((o1.x + o1.y) * h, (o1.y - o1.x) * h);
    p2 = make_float2(o2.y, -o2.x);
    p3 = make_float2((o3.y - o3.x) * h, -(o3.x + o3.y) * h);
  } else {
    p1 = make_float2((o1.x - o1.y) * h, (o1.x + o1.y) * h);
    p2 = make_float2(-o2.y, o2.x);
    p3 = make_float2(-(o3.x + o3.y) * h, (o3.x - o3.y) * h);
  }
  v[0] = cadd(e0, o0); v[4] = csub(e0, o0);
  v[1] = cadd(e1, p1); v[5] = csub(e1, p1);
  v[2] = cadd(e2, p2); v[6] = csub(e2, p2);
  v[3] = cadd(e3, p3); v[7] = csub(e3, p3);
}

__device__ __forceinline__ float wave_sum(float v) {
#define DPP_ADD(ctrl, rmask) v += __int_as_float(__builtin_amdgcn_update_dpp(0, __float_as_int(v), ctrl, rmask, 0xf, false))
  DPP_ADD(0x111, 0xf); DPP_ADD(0x112, 0xf); DPP_ADD(0x114, 0xf); DPP_ADD(0x118, 0xf);
  DPP_ADD(0x142, 0xa); DPP_ADD(0x143, 0xc);
#undef DPP_ADD
  return __int_as_float(__builtin_amdgcn_readlane(__float_as_int(v), 63));
}

#define WSYNC() do { __builtin_amdgcn_fence(__ATOMIC_RELEASE, "wavefront"); __builtin_amdgcn_s_waitcnt(0xC07F); __builtin_amdgcn_wave_barrier(); __builtin_amdgcn_fence(__ATOMIC_ACQUIRE, "wavefront"); } while (0)

__host__ __device__ __forceinline__ int wslot(int j) {
  int q1 = j & 7, q2 = (j >> 3) & 7, q3 = (j >> 6) & 7, q4 = j >> 9;
  int u = q1 * 64 + q2 * 8 + q3;
  return ((u >> 8) * 4 + q4) * 256 + (u & 255);
}

constexpr int K0_ROWBLK = 256, K0_WTBLK = 128, K0_PFBLK = 224, K0_SPBLK = 32;
__global__ __launch_bounds__(512) void k0_ln_rowfft(const u16* __restrict__ x, u16* __restrict__ xn,
                                                    float2* __restrict__ XT, const u16* __restrict__ wr,
                                                    const u16* __restrict__ wi, u32* __restrict__ WT,
                                                    float2* __restrict__ W0h, float2* __restrict__ W256h,
                                                    const u16* __restrict__ pf0, const u16* __restrict__ pf1,
                                                    const u16* __restrict__ pf2) {
  __shared__ __attribute__((aligned(16))) float2 sm[8 * 260 + 2048 + 64];
  const int tid = threadIdx.x, lane = tid & 63, wv = tid >> 6;
  const int b = blockIdx.x;
  if (b >= K0_ROWBLK) {
    int a = b - K0_ROWBLK;
    if (a < K0_WTBLK) {
      u32* tile = reinterpret_cast<u32*>(sm);
      int q1 = a & 7, q4 = (a >> 3) & 3, kq = a >> 5;
      u32 re[8], im[8];
#pragma unroll
      for (int e = 0; e < 8; ++e) {
        int idx = tid + 512 * e, i = idx >> 6, kk = idx & 63;
        int j = q1 + 512 * q4 + 8 * i;
        re[e] = wr[j * DF_ + 64 * kq + kk];
        im[e] = wi[j * DF_ + 64 * kq + kk];
      }
#pragma unroll
      for (int e = 0; e < 8; ++e) {
        int idx = tid + 512 * e, i = idx >> 6, kk = idx & 63;
        tile[i * 65 + kk] = re[e] | (im[e] << 16);
      }
      __syncthreads();
      int base = ((q1 >> 2) * 4 + q4) * 256 + (q1 & 3) * 64;
#pragma unroll
      for (int e = 0; e < 8; ++e) {
        int idx = tid + 512 * e, kk = idx >> 6, o = idx & 63;
        int q2 = o >> 3, q3 = o & 7;
        int i = q2 + 8 * q3;
        int k = 64 * kq + kk;
        if (k > 0) WT[k * 2048 + base + o] = tile[i * 65 + kk];
      }
    } else if (a >= K0_WTBLK + K0_SPBLK) {
      const int pb = a - K0_WTBLK - K0_SPBLK;
      const uint4* srcs[3] = {reinterpret_cast<const uint4*>(pf0), reinterpret_cast<const uint4*>(pf1),
                              reinterpret_cast<const uint4*>(pf2)};
      u32 acc = 0;
      const int per = (D_ * F_ * 2 / 16) / K0_PFBLK;
#pragma unroll
      for (int mth = 0; mth < 3; ++mth) {
#pragma unroll
        for (int e = tid; e < per; e += 512) {
          uint4 v = srcs[mth][pb * per + e];
          acc ^= v.x ^ v.y ^ v.z ^ v.w;
        }
      }
      asm volatile("" ::"v"(acc));
    } else {
      if (tid < 64) {
        int j = tid + 64 * (a - K0_WTBLK);
        int jm = (2048 - j) & 2047;
        float a0r = bf2f(wr[j * DF_]), a0i = bf2f(wi[j * DF_]);
        float b0r = bf2f(wr[jm * DF_]), b0i = bf2f(wi[jm * DF_]);
        float a1r = bf2f(wr[j * DF_ + 256]), a1i = bf2f(wi[j * DF_ + 256]);
        float b1r = bf2f(wr[jm * DF_ + 256]), b1i = bf2f(wi[jm * DF_ + 256]);
        int sl = wslot(j);
        W0h[sl] = make_float2(0.5f * (a0r + b0r), 0.5f * (a0i - b0i));
        W256h[sl] = make_float2(0.5f * (a1r + b1r), 0.5f * (a1i - b1i));
      }
    }
    return;
  }
  float2* ex = sm + wv * 260;
  float2* ot = sm + 8 * 260;
  const float2 tA1 = tw(8 * lane), tA2 = tw(16 * lane), tA3 = tw(24 * lane);
  const float2 tB1 = tw(32 * (lane & 15)), tB2 = tw(64 * (lane & 15)), tB3 = tw(96 * (lane & 15));
  const float2 tC1 = tw(128 * (lane & 3)), tC2 = tw(256 * (lane & 3)), tC3 = tw(384 * (lane & 3));
  float2 tP[4];
#pragma unroll
  for (int i = 0; i < 4; ++i) tP[i] = tw(4 * (lane + 64 * i));
  float2 z[4];
  {
    const int row = b * 8 + wv;
    const u32* xr = reinterpret_cast<const u32*>(x + row * D_);
    u32 v[4];
#pragma unroll
    for (int m = 0; m < 4; ++m) v[m] = xr[lane + 64 * m];
    float f[8];
    float sum = 0.f;
#pragma unroll
    for (int m = 0; m < 4; ++m) { f[2 * m] = bflo(v[m]); f[2 * m + 1] = bfhi(v[m]); sum += f[2 * m] + f[2 * m + 1]; }
    sum = wave_sum(sum);
    float mean = sum * (1.0f / 512.0f);
    float sq = 0.f;
#pragma unroll
    for (int e = 0; e < 8; ++e) { float d = f[e] - mean; sq = fmaf(d, d, sq); }
    sq = wave_sum(sq);
    float var = sq * (1.0f / 512.0f);
    float mb = rbf(mean), vb = rbf(var);
    float den = rbf(sqrtf(rbf(vb + 1e-5f)));
    float rc = __builtin_amdgcn_rcpf(den);
    rc = fmaf(fmaf(-den, rc, 1.0f), rc, rc);
    u32* xo = reinterpret_cast<u32*>(xn + row * D_);
#pragma unroll
    for (int m = 0; m < 4; ++m) {
      float d0 = rbf(f[2 * m] - mb), d1 = rbf(f[2 * m + 1] - mb);
      float q0 = d0 * rc, q1 = d1 * rc;
      q0 = fmaf(fmaf(-q0, den, d0), rc, q0);
      q1 = fmaf(fmaf(-q1, den, d1), rc, q1);
      u32 pk = pack2(q0, q1);
      xo[lane + 64 * m] = pk;
      z[m] = make_float2(bflo(pk), bfhi(pk));
    }
  }
  bfly4<false>(z[0], z[1], z[2], z[3]);
  z[1] = cmul(z[1], tA1); z[2] = cmul(z[2], tA2); z[3] = cmul(z[3], tA3);
#pragma unroll
  for (int q = 0; q < 4; ++q) ex[q * 64 + lane] = z[q];
  WSYNC();
  {
    int q1 = lane >> 4, n1 = lane & 15;
#pragma unroll
    for (int m = 0; m < 4; ++m) z[m] = ex[q1 * 64 + n1 + 16 * m];
    bfly4<false>(z[0], z[1], z[2], z[3]);
    z[1] = cmul(z[1], tB1); z[2] = cmul(z[2], tB2); z[3] = cmul(z[3], tB3);
    WSYNC();
#pragma unroll
    for (int q = 0; q < 4; ++q) ex[q1 * 64 + q * 16 + n1] = z[q];
  }
  WSYNC();
  {
    int q12 = lane >> 2, n2 = lane & 3;
#pragma unroll
    for (int m = 0; m < 4; ++m) z[m] = ex[q12 * 16 + n2 + 4 * m];
    bfly4<false>(z[0], z[1], z[2], z[3]);
    z[1] = cmul(z[1], tC1); z[2] = cmul(z[2], tC2); z[3] = cmul(z[3], tC3);
    WSYNC();
#pragma unroll
    for (int q = 0; q < 4; ++q) ex[q12 * 16 + q * 4 + n2] = z[q];
  }
  WSYNC();
  {
    int kb = (lane >> 4) + 4 * ((lane >> 2) & 3) + 16 * (lane & 3);
#pragma unroll
    for (int m = 0; m < 4; ++m) z[m] = ex[lane * 4 + m];
    bfly4<false>(z[0], z[1], z[2], z[3]);
    WSYNC();
#pragma unroll
    for (int q = 0; q < 4; ++q) ex[kb + 64 * q] = z[q];
  }
  WSYNC();
#pragma unroll
  for (int i = 0; i < 4; ++i) {
    int k = lane + 64 * i;
    float2 zk = ex[k], zm = ex[(256 - k) & 255];
    float2 E = make_float2(0.5f * (zk.x + zm.x), 0.5f * (zk.y - zm.y));
    float2 Dd = make_float2(zk.x - zm.x, zk.y + zm.y);
    float2 O = make_float2(0.5f * Dd.y, -0.5f * Dd.x);
    float2 X = cadd(E, cmul(O, tP[i]));
    if (k == 0) X = make_float2(zk.x + zk.y, zk.x - zk.y);
    ot[k * 8 + (wv ^ (k & 7))] = X;
  }
  __syncthreads();
  {
    int k = tid >> 1, hf = tid & 1;
    float4* dst = reinterpret_cast<float4*>(XT + (size_t)(b >> 1) * 4096 + k * 16 + (b & 1) * 8 + hf * 4);
#pragma unroll
    for (int j = 0; j < 2; ++j) {
      int r0 = hf * 4 + 2 * j;
      float2 a2 = ot[k * 8 + (r0 ^ (k & 7))], c2 = ot[k * 8 + ((r0 + 1) ^ (k & 7))];
      dst[j] = make_float4(a2.x, a2.y, c2.x, c2.y);
    }
  }
}

__device__ __forceinline__ void colfft_block(int k, const float2* __restrict__ XT, float2* __restrict__ YT,
                                             const u32* __restrict__ WT, const float2* __restrict__ W0h,
                                             const float2* __restrict__ W256h, float2* lds) {
  float2* bufA = lds;
  float2* bufB = lds + 2304;
  const int t = threadIdx.x;
  float2 v[8];
#pragma unroll
  for (int m = 0; m < 8; ++m) {
    int s = t + 256 * m;
    v[m] = XT[(size_t)(s >> 4) * 4096 + k * 16 + (s & 15)];
  }
  float2 tw1[7], tw2[7], tw3[7];
  const int q1 = t >> 5, n1 = t & 31, n2 = t & 3, q2r = (t >> 2) & 7;
#pragma unroll
  for (int q = 1; q < 8; ++q) {
    tw1[q - 1] = tw((t * q) & 2047);
    tw2[q - 1] = tw((8 * n1 * q) & 2047);
    tw3[q - 1] = tw((64 * n2 * q) & 2047);
  }
  bfly8<false>(v);
#pragma unroll
  for (int q = 1; q < 8; ++q) v[q] = cmul(v[q], tw1[q - 1]);
#pragma unroll
  for (int q = 0; q < 8; ++q) bufA[q * 256 + t] = v[q];
  __syncthreads();
#pragma unroll
  for (int m = 0; m < 8; ++m) v[m] = bufA[q1 * 256 + n1 + 32 * m];
  bfly8<false>(v);
#pragma unroll
  for (int q = 1; q < 8; ++q) v[q] = cmul(v[q], tw2[q - 1]);
#pragma unroll
  for (int q = 0; q < 8; ++q) bufB[q1 * 288 + q * 36 + n1] = v[q];
  __syncthreads();
#pragma unroll
  for (int m = 0; m < 8; ++m) v[m] = bufB[q1 * 288 + q2r * 36 + n2 + 4 * m];
  bfly8<false>(v);
#pragma unroll
  for (int q = 1; q < 8; ++q) v[q] = cmul(v[q], tw3[q - 1]);
#pragma unroll
  for (int q = 0; q < 8; ++q) bufA[(q1 * 8 + q2r) * 36 + q * 4 + n2] = v[q];
  __syncthreads();
#pragma unroll
  for (int h = 0; h < 2; ++h) {
    int u = t + 256 * h;
#pragma unroll
    for (int m = 0; m < 4; ++m) v[4 * h + m] = bufA[(u >> 3) * 36 + (u & 7) * 4 + m];
  }
  bfly4<false>(v[0], v[1], v[2], v[3]);
  bfly4<false>(v[4], v[5], v[6], v[7]);
  const float scale = 1.0f / (2048.0f * 512.0f);
  if (k != 0) {
#pragma unroll
    for (int i = 0; i < 8; ++i) {
      u32 w = WT[k * 2048 + i * 256 + t];
      float2 wf = make_float2(bflo(w) * scale, bfhi(w) * scale);
      v[i] = cmul(v[i], wf);
    }
  } else {
    int jj[8];
#pragma unroll
    for (int i = 0; i < 8; ++i) {
      int u = t + 256 * (i >> 2), q4 = i & 3;
      int a1 = u >> 6, a2 = (u >> 3) & 7, a3 = u & 7;
      jj[i] = a1 + 8 * a2 + 64 * a3 + 512 * q4;
      bufB[jj[i]] = v[i];
    }
    __syncthreads();
#pragma unroll
    for (int i = 0; i < 8; ++i) {
      float2 zk = v[i], zm = bufB[(2048 - jj[i]) & 2047];
      float2 A = make_float2(0.5f * (zk.x + zm.x), 0.5f * (zk.y - zm.y));
      float2 Dd = make_float2(zk.x - zm.x, zk.y + zm.y);
      float2 Bc = make_float2(0.5f * Dd.y, -0.5f * Dd.x);
      float2 w0 = W0h[i * 256 + t], w1 = W256h[i * 256 + t];
      float2 pa = cmul(A, w0), pb = cmul(Bc, w1);
      v[i] = make_float2((pa.x - pb.y) * scale, (pa.y + pb.x) * scale);
    }
    __syncthreads();
  }
  bfly4<true>(v[0], v[1], v[2], v[3]);
  bfly4<true>(v[4], v[5], v[6], v[7]);
#pragma unroll
  for (int h = 0; h < 2; ++h) {
    int u = t + 256 * h;
#pragma unroll
    for (int m = 0; m < 4; ++m) bufB[(u >> 3) * 36 + (u & 7) * 4 + m] = v[4 * h + m];
  }
  __syncthreads();
#pragma unroll
  for (int q = 0; q < 8; ++q) v[q] = bufB[(q1 * 8 + q2r) * 36 + q * 4 + n2];
#pragma unroll
  for (int q = 1; q < 8; ++q) v[q] = cmulc(v[q], tw3[q - 1]);
  bfly8<true>(v);
#pragma unroll
  for (int m = 0; m < 8; ++m) bufA[q1 * 288 + q2r * 36 + n2 + 4 * m] = v[m];
  __syncthreads();
#pragma unroll
  for (int q = 0; q < 8; ++q) v[q] = bufA[q1 * 288 + q * 36 + n1];
#pragma unroll
  for (int q = 1; q < 8; ++q) v[q] = cmulc(v[q], tw2[q - 1]);
  bfly8<true>(v);
#pragma unroll
  for (int m = 0; m < 8; ++m) bufB[q1 * 256 + n1 + 32 * m] = v[m];
  __syncthreads();
#pragma unroll
  for (int q = 0; q < 8; ++q) v[q] = bufB[q * 256 + t];
#pragma unroll
  for (int q = 1; q < 8; ++q) v[q] = cmulc(v[q], tw1[q - 1]);
  bfly8<true>(v);
#pragma unroll
  for (int m = 0; m < 8; ++m) {
    int s = t + 256 * m;
    YT[(size_t)(s >> 4) * 4096 + k * 16 + (s & 15)] = v[m];
  }
}


__device__ __forceinline__ void rowifft_block(int b, const float2* __restrict__ YT, u16* __restrict__ sout, float2* lds,
                                              const u16* __restrict__ xin, const u16* __restrict__ gin) {
  const int tid = threadIdx.x, lane = tid & 63, wv = tid >> 6;
  float2* it = lds;
  float2* ex = lds + 2048 + wv * 260;
  const float2 tA1 = tw(8 * lane), tA2 = tw(16 * lane), tA3 = tw(24 * lane);
  const float2 tB1 = tw(32 * (lane & 15)), tB2 = tw(64 * (lane & 15)), tB3 = tw(96 * (lane & 15));
  const float2 tC1 = tw(128 * (lane & 3)), tC2 = tw(256 * (lane & 3)), tC3 = tw(384 * (lane & 3));
  const int kb = (lane >> 4) + 4 * ((lane >> 2) & 3) + 16 * (lane & 3);
  float2 tP[4];
#pragma unroll
  for (int q = 0; q < 4; ++q) tP[q] = tw(4 * (kb + 64 * q));
  const int row = b * 8 + wv;
  u32 xg[8];
#pragma unroll
  for (int m = 0; m < 4; ++m) {
    xg[m] = reinterpret_cast<const u32*>(xin + row * D_)[lane + 64 * m];
    xg[4 + m] = reinterpret_cast<const u32*>(gin + row * D_)[lane + 64 * m];
  }
  {
    int k = tid >> 1, hf = tid & 1;
    const float4* src = reinterpret_cast<const float4*>(YT + (size_t)(b >> 1) * 4096 + k * 16 + (b & 1) * 8 + hf * 4);
    float4 d0 = src[0], d1 = src[1];
    int r0 = hf * 4;
    it[k * 8 + ((r0 + 0) ^ (k & 7))] = make_float2(d0.x, d0.y);
    it[k * 8 + ((r0 + 1) ^ (k & 7))] = make_float2(d0.z, d0.w);
    it[k * 8 + ((r0 + 2) ^ (k & 7))] = make_float2(d1.x, d1.y);
    it[k * 8 + ((r0 + 3) ^ (k & 7))] = make_float2(d1.z, d1.w);
  }
  __syncthreads();
  float2 z[4];
#pragma unroll
  for (int q = 0; q < 4; ++q) {
    int k = kb + 64 * q, km = (256 - k) & 255;
    float2 xk = it[k * 8 + (wv ^ (k & 7))], xm = it[km * 8 + (wv ^ (km & 7))];
    float2 E = make_float2(0.5f * (xk.x + xm.x), 0.5f * (xk.y - xm.y));
    float2 Dd = make_float2(0.5f * (xk.x - xm.x), 0.5f * (xk.y + xm.y));
    float2 O = cmulc(Dd, tP[q]);
    float2 Z = make_float2(E.x - O.y, E.y + O.x);
    if (k == 0) Z = make_float2(0.5f * (xk.x + xk.y), 0.5f * (xk.x - xk.y));
    z[q] = Z;
  }
  bfly4<true>(z[0], z[1], z[2], z[3]);
#pragma unroll
  for (int m = 0; m < 4; ++m) ex[lane * 4 + m] = z[m];
  WSYNC();
  {
    int q12 = lane >> 2, n2 = lane & 3;
#pragma unroll
    for (int q = 0; q < 4; ++q) z[q] = ex[q12 * 16 + q * 4 + n2];
    z[1] = cmulc(z[1], tC1); z[2] = cmulc(z[2], tC2); z[3] = cmulc(z[3], tC3);
    bfly4<true>(z[0], z[1], z[2], z[3]);
    WSYNC();
#pragma unroll
    for (int m = 0; m < 4; ++m) ex[q12 * 16 + n2 + 4 * m] = z[m];
  }
  WSYNC();
  {
    int q1 = lane >> 4, n1 = lane & 15;
#pragma unroll
    for (int q = 0; q < 4; ++q) z[q] = ex[q1 * 64 + q * 16 + n1];
    z[1] = cmulc(z[1], tB1); z[2] = cmulc(z[2], tB2); z[3] = cmulc(z[3], tB3);
    bfly4<true>(z[0], z[1], z[2], z[3]);
    WSYNC();
#pragma unroll
    for (int m = 0; m < 4; ++m) ex[q1 * 64 + n1 + 16 * m] = z[m];
  }
  WSYNC();
#pragma unroll
  for (int q = 0; q < 4; ++q) z[q] = ex[q * 64 + lane];
  z[1] = cmulc(z[1], tA1); z[2] = cmulc(z[2], tA2); z[3] = cmulc(z[3], tA3);
  bfly4<true>(z[0], z[1], z[2], z[3]);
  u32* so = reinterpret_cast<u32*>(sout + row * D_);
#pragma unroll
  for (int m = 0; m < 4; ++m) {
    u32 xv = xg[m], gv = xg[4 + m];
    float s0 = rbf(2.0f * z[m].x), s1 = rbf(2.0f * z[m].y);
    float p0 = rbf(s0 * bflo(gv)), p1 = rbf(s1 * bfhi(gv));
    so[lane + 64 * m] = pack2(bflo(xv) + p0, bfhi(xv) + p1);
  }
}

constexpr int G1_A_BYTES = 256 * 64, G1_B_BYTES = 32 * 256, G1_STAGE = G1_A_BYTES + G1_B_BYTES, G1_NK = D_ / 32;
constexpr int G1_LDS = 3 * G1_STAGE;
#define WAIT_V(n) asm volatile("s_waitcnt vmcnt(%0)" ::"n"(n) : "memory")
#define WAIT_L(n) asm volatile("s_waitcnt lgkmcnt(%0)" ::"n"(n) : "memory")
typedef __attribute__((address_space(3))) s16x4* lds_s16x4_p;
__device__ __forceinline__ s16x4 tr_read(const char* p) {
  return __builtin_amdgcn_ds_read_tr16_b64_v4i16((lds_s16x4_p)(p));
}
__device__ __forceinline__ void gemm1_block(int tm, int tn, const u16* __restrict__ xn, const u16* __restrict__ wg,
                                            const u16* __restrict__ wu, u16* __restrict__ h, char* lds) {
  const int tid = threadIdx.x, lane = tid & 63, w = __builtin_amdgcn_readfirstlane(tid >> 6);
  const int m0 = tm * 256, f0 = tn * 64;
  const int HT = 0x1320;
  const int arow = lane >> 2, aslot = lane & 3;
  const int ach = aslot ^ ((HT >> (4 * ((arow >> 2) & 3))) & 3);
  const u16* asrc = xn + (size_t)(m0 + 64 * w + arow) * D_ + 8 * ach;
  const int bphys = lane >> 1, bhf = lane & 1;
  const int bk = bphys ^ (((bphys >> 3) & 1) << 2);
  const u16* bbase = (w < 2 ? wg : wu) + (size_t)bk * F_ + f0 + 32 * (w & 1) + 8 * bhf;
  auto issue3 = [&](int t, int buf) {
    char* st = lds + buf * G1_STAGE;
    const int k0 = t * 32;
#pragma unroll
    for (int i = 0; i < 4; ++i)
      __builtin_amdgcn_global_load_lds((const u32*)(asrc + (size_t)i * 16 * D_ + k0), (u32*)(st + (64 * w + 16 * i) * 64), 16, 0, 0);
#pragma unroll
    for (int i = 0; i < 2; ++i)
      __builtin_amdgcn_global_load_lds((const u32*)(bbase + (size_t)k0 * F_ + 16 * i), (u32*)(st + G1_A_BYTES + (2 * w + i) * 1024), 16, 0, 0);
  };
  f32x4 acc[4][8];
#pragma unroll
  for (int a = 0; a < 4; ++a)
#pragma unroll
    for (int b = 0; b < 8; ++b) acc[a][b] = f32x4{0.f, 0.f, 0.f, 0.f};
  const int fr = lane & 15, fq = lane >> 4;
  const int aoff = (64 * w + fr) * 64 + 16 * (fq ^ ((HT >> (4 * ((fr >> 2) & 3))) & 3));
  const int g = lane >> 4, q = (lane & 15) >> 2, p = lane & 3;
  const int boff0 = G1_A_BYTES + ((8 * g + 4 * (0 ^ (g & 1)) + q) * 32) + p * 8;
  const int boff1 = G1_A_BYTES + ((8 * g + 4 * (1 ^ (g & 1)) + q) * 32) + p * 8;
  #define RD_A(dst, st) _Pragma("unroll") for (int mi = 0; mi < 4; ++mi) dst[mi] = *reinterpret_cast<const bf16x8*>((st) + aoff + mi * 1024)
  #define RD_B(dst, st, n0) _Pragma("unroll") for (int ni = 0; ni < 4; ++ni) { \
      s16x4 lo = tr_read((st) + boff0 + (n0 + ni) * 1024), hi = tr_read((st) + boff1 + (n0 + ni) * 1024); \
      dst[ni] = bf16x8{lo[0], lo[1], lo[2], lo[3], hi[0], hi[1], hi[2], hi[3]}; }
  #define MMA(Bf, Af, n0) _Pragma("unroll") for (int mi = 0; mi < 4; ++mi) _Pragma("unroll") for (int ni = 0; ni < 4; ++ni) \
      acc[mi][n0 + ni] = __builtin_amdgcn_mfma_f32_16x16x32_bf16(Bf[ni], Af[mi], acc[mi][n0 + ni], 0, 0, 0)
  #define SCHED() __builtin_amdgcn_sched_barrier(0)
  bf16x8 afA[4], afB[4], bgA[4], bgB[4], bu[4];
  issue3(0, 0); issue3(1, 1); issue3(2, 2);
  WAIT_V(12);
  __builtin_amdgcn_s_barrier();
  RD_A(afA, lds); RD_B(bgA, lds, 0);
  int cur = 0;
  for (int t = 0; t < G1_NK; t += 2) {
    {
      const char* st = lds + cur * G1_STAGE;
      int nxt = cur == 2 ? 0 : cur + 1;
      const char* sn = lds + nxt * G1_STAGE;
      __builtin_amdgcn_s_waitcnt(0xC07F);
      RD_B(bu, st, 4);
      SCHED();
      __builtin_amdgcn_s_setprio(1);
      MMA(bgA, afA, 0);
      __builtin_amdgcn_s_setprio(0);
      SCHED();
      if (t + 2 < G1_NK) { WAIT_V(6); } else { WAIT_V(0); }
      WAIT_L(0);
      __builtin_amdgcn_s_barrier();
      if (t + 3 < G1_NK) issue3(t + 3, cur);
      RD_A(afB, sn); RD_B(bgB, sn, 0);
      SCHED();
      __builtin_amdgcn_s_setprio(1);
      MMA(bu, afA, 4);
      __builtin_amdgcn_s_setprio(0);
      SCHED();
      cur = nxt;
    }
    {
      const char* st = lds + cur * G1_STAGE;
      int nxt = cur == 2 ? 0 : cur + 1;
      const char* sn = lds + nxt * G1_STAGE;
      __builtin_amdgcn_s_waitcnt(0xC07F);
      RD_B(bu, st, 4);
      SCHED();
      __builtin_amdgcn_s_setprio(1);
      MMA(bgB, afB, 0);
      __builtin_amdgcn_s_setprio(0);
      SCHED();
      if (t + 3 < G1_NK) { WAIT_V(6); } else { WAIT_V(0); }
      WAIT_L(0);
      __builtin_amdgcn_s_barrier();
      if (t + 4 < G1_NK) issue3(t + 4, cur);
      if (t + 2 < G1_NK) { RD_A(afA, sn); RD_B(bgA, sn, 0); }
      SCHED();
      __builtin_amdgcn_s_setprio(1);
      MMA(bu, afB, 4);
      __builtin_amdgcn_s_setprio(0);
      SCHED();
      cur = nxt;
    }
  }
#pragma unroll
  for (int mi = 0; mi < 4; ++mi) {
    const int row = m0 + 64 * w + 16 * mi + fr;
#pragma unroll
    for (int ni = 0; ni < 4; ++ni) {
      float o[4];
#pragma unroll
      for (int j = 0; j < 4; ++j) {
        float gg = rbf(acc[mi][ni][j]), u = rbf(acc[mi][ni + 4][j]);
        float e = __builtin_amdgcn_exp2f(gg * -1.4426950408889634f);
        o[j] = rbf(gg * __builtin_amdgcn_rcpf(1.0f + e)) * u;
      }
      uint2 pk = make_uint2(pack2(o[0], o[1]), pack2(o[2], o[3]));
      *reinterpret_cast<uint2*>(h + (size_t)row * F_ + f0 + 16 * ni + 4 * fq) = pk;
    }
  }
}

constexpr int G2_A_BYTES = 64 * 128, G2_STAGE = 16384, G2_NK = F_ / 64;
constexpr int G2_NST = 4;
constexpr int G2_LDS = G2_NST * G2_STAGE;
__device__ __forceinline__ int g2swz(int row) { return (row & 3) | (((row >> 3) & 1) << 2); }
__device__ __forceinline__ void gemm2_block(int tm, int tn, const u16* __restrict__ h, const u16* __restrict__ wd,
                                            u16* __restrict__ gout, char* lds) {
  const int tid = threadIdx.x, lane = tid & 63, w = __builtin_amdgcn_readfirstlane(tid >> 6);
  const int m0 = tm * 64, n0 = tn * 64, wm = w >> 1, wn = w & 1;
  const int ar = lane >> 3, as = lane & 7;
  const u16* asrc0 = h + (size_t)(m0 + 16 * w + ar) * F_ + 8 * (as ^ g2swz(ar));
  const u16* asrc1 = h + (size_t)(m0 + 16 * w + 8 + ar) * F_ + 8 * (as ^ g2swz(8 + ar));
  const u16* bsrc0 = wd + (size_t)(16 * w + ar) * D_ + n0 + 8 * (as ^ g2swz(ar));
  const u16* bsrc1 = wd + (size_t)(16 * w + 8 + ar) * D_ + n0 + 8 * (as ^ g2swz(8 + ar));
  auto issue = [&](int t, int buf) {
    char* st = lds + buf * G2_STAGE;
    const int k0 = t * 64;
    __builtin_amdgcn_global_load_lds((const u32*)(asrc0 + k0), (u32*)(st + (16 * w) * 128), 16, 0, 0);
    __builtin_amdgcn_global_load_lds((const u32*)(asrc1 + k0), (u32*)(st + (16 * w + 8) * 128), 16, 0, 0);
    __builtin_amdgcn_global_load_lds((const u32*)(bsrc0 + (size_t)k0 * D_), (u32*)(st + G2_A_BYTES + (16 * w) * 128), 16, 0, 0);
    __builtin_amdgcn_global_load_lds((const u32*)(bsrc1 + (size_t)k0 * D_), (u32*)(st + G2_A_BYTES + (16 * w + 8) * 128), 16, 0, 0);
  };
  f32x4 acc[2][2];
#pragma unroll
  for (int a = 0; a < 2; ++a)
#pragma unroll
    for (int b = 0; b < 2; ++b) acc[a][b] = f32x4{0.f, 0.f, 0.f, 0.f};
  const int fr = lane & 15, fq = lane >> 4;
  const int arow = 32 * wm + fr;
  const int aoff0 = arow * 128 + 16 * ((0 + fq) ^ g2swz(fr));
  const int aoff1 = arow * 128 + 16 * ((4 + fq) ^ g2swz(fr));
  const int g = lane >> 4, q = (lane & 15) >> 2, p = lane & 3;
  const int bsw = q | ((g & 1) << 2);
  const int boffj0 = G2_A_BYTES + (8 * g + q) * 128 + 16 * ((4 * wn + 0 + (p >> 1)) ^ bsw) + 8 * (p & 1);
  const int boffj1 = G2_A_BYTES + (8 * g + q) * 128 + 16 * ((4 * wn + 2 + (p >> 1)) ^ bsw) + 8 * (p & 1);
  #define G2_RD(AF, BF, st) do { \
    _Pragma("unroll") for (int mi = 0; mi < 2; ++mi) { \
      AF[0][mi] = *reinterpret_cast<const bf16x8*>((st) + aoff0 + mi * 2048); \
      AF[1][mi] = *reinterpret_cast<const bf16x8*>((st) + aoff1 + mi * 2048); } \
    _Pragma("unroll") for (int ks = 0; ks < 2; ++ks) _Pragma("unroll") for (int j = 0; j < 2; ++j) { \
        s16x4 lo = tr_read((st) + (j ? boffj1 : boffj0) + ks * 4096), hi = tr_read((st) + (j ? boffj1 : boffj0) + ks * 4096 + 512); \
        BF[ks][j] = bf16x8{lo[0], lo[1], lo[2], lo[3], hi[0], hi[1], hi[2], hi[3]}; } } while (0)
  #define G2_MMA(AF, BF) _Pragma("unroll") for (int ks = 0; ks < 2; ++ks) _Pragma("unroll") for (int mi = 0; mi < 2; ++mi) \
      _Pragma("unroll") for (int j = 0; j < 2; ++j) \
          acc[mi][j] = __builtin_amdgcn_mfma_f32_16x16x32_bf16(BF[ks][j], AF[ks][mi], acc[mi][j], 0, 0, 0)
  bf16x8 afA[2][2], bfA[2][2], afB[2][2], bfB[2][2];
#pragma unroll
  for (int i = 0; i < G2_NST; ++i) issue(i, i);
  WAIT_V(4 * (G2_NST - 1));
  __builtin_amdgcn_s_barrier();
  G2_RD(afA, bfA, lds);
  int cur = 0;
  for (int t = 0; t < G2_NK; t += 2) {
    {
      int nxt = cur == G2_NST - 1 ? 0 : cur + 1;
      if (t + G2_NST - 1 < G2_NK) { WAIT_V(4 * (G2_NST - 2)); } else { WAIT_V(0); }
      WAIT_L(0);
      __builtin_amdgcn_s_barrier();
      if (t + G2_NST < G2_NK) issue(t + G2_NST, cur);
      G2_RD(afB, bfB, lds + nxt * G2_STAGE);
      SCHED();
      G2_MMA(afA, bfA);
      SCHED();
      cur = nxt;
    }
    {
      int nxt = cur == G2_NST - 1 ? 0 : cur + 1;
      if (t + G2_NST < G2_NK) { WAIT_V(4 * (G2_NST - 2)); } else { WAIT_V(0); }
      WAIT_L(0);
      __builtin_amdgcn_s_barrier();
      if (t + 1 + G2_NST < G2_NK) issue(t + 1 + G2_NST, cur);
      if (t + 2 < G2_NK) G2_RD(afA, bfA, lds + nxt * G2_STAGE);
      SCHED();
      G2_MMA(afB, bfB);
      SCHED();
      cur = nxt;
    }
  }
#pragma unroll
  for (int mi = 0; mi < 2; ++mi) {
    const int row = m0 + 32 * wm + 16 * mi + fr;
#pragma unroll
    for (int j = 0; j < 2; ++j) {
      float o[4];
#pragma unroll
      for (int e = 0; e < 4; ++e) {
        float f = rbf(acc[mi][j][e]);
        float ex = __builtin_amdgcn_exp2f(f * -1.4426950408889634f);
        o[e] = f * __builtin_amdgcn_rcpf(1.0f + ex);
      }
      uint2 pk = make_uint2(pack2(o[0], o[1]), pack2(o[2], o[3]));
      *reinterpret_cast<uint2*>(gout + (size_t)row * D_ + n0 + 32 * wn + 16 * j + 4 * fq) = pk;
    }
  }
}

__global__ __launch_bounds__(256, 2) void k1_fused(const u16* __restrict__ xn, const u16* __restrict__ wg,
                                                   const u16* __restrict__ wu, u16* __restrict__ h,
                                                   const float2* __restrict__ XT, float2* __restrict__ YT,
                                                   const u32* __restrict__ WT, const float2* __restrict__ W0h,
                                                   const float2* __restrict__ W256h) {
  __shared__ __attribute__((aligned(1024))) char lds[G1_LDS];
  const int b = blockIdx.x;
  if (b < 224) {
    int xcd = b & 7, idx = b >> 3, tm, tn;
    if (idx < 24) { tm = idx & 7; tn = xcd + 8 * (idx >> 3); }
    else { tn = 24 + (xcd >> 1); tm = 4 * (xcd & 1) + (idx - 24); }
    gemm1_block(tm, tn, xn, wg, wu, h, lds);
  } else {
    colfft_block(b - 224, XT, YT, WT, W0h, W256h, reinterpret_cast<float2*>(lds));
  }
}
__global__ __launch_bounds__(256) void k2_gemm2(const u16* __restrict__ h, const u16* __restrict__ wd,
                                                   u16* __restrict__ g) {
  __shared__ __attribute__((aligned(1024))) char lds[G2_LDS];
  const int b = blockIdx.x, xcd = b & 7, idx = b >> 3;
  gemm2_block(xcd * 4 + (idx >> 3), idx & 7, h, wd, g, lds);
}
__global__ __launch_bounds__(512) void k3_final(const float2* __restrict__ YT, const u16* __restrict__ x,
                                                const u16* __restrict__ g, u16* __restrict__ out) {
  __shared__ __attribute__((aligned(16))) float2 lds[2048 + 8 * 260];
  rowifft_block(blockIdx.x, YT, out, lds, x, g);
}

extern "C" void kernel_launch(void* const* d_in, const int* in_sizes, int n_in, void* d_out, int out_size,
                              void* d_ws, size_t ws_size, hipStream_t stream) {
  const u16* x = (const u16*)d_in[0];
  const u16* wr = (const u16*)d_in[1];
  const u16* wi = (const u16*)d_in[2];
  const u16* wg = (const u16*)d_in[4];
  const u16* wu = (const u16*)d_in[5];
  const u16* wd = (const u16*)d_in[6];
  u16* out = (u16*)d_out;
  char* ws = (char*)d_ws;
  u16* xn = (u16*)(ws + 0);
  float2* XT = (float2*)(ws + (2u << 20));
  float2* YT = (float2*)(ws + (6u << 20));
  u32* WT = (u32*)(ws + (10u << 20));
  float2* W0h = (float2*)(ws + (12u << 20));
  float2* W256h = (float2*)(ws + (12u << 20) + 65536);
  u16* h = (u16*)(ws + (13u << 20));
  u16* g = (u16*)(ws + (21u << 20));
  u16* sb = (u16*)(ws + (23u << 20));

  k0_ln_rowfft<<<K0_ROWBLK + K0_WTBLK + K0_SPBLK + K0_PFBLK, 512, 0, stream>>>(x, xn, XT, wr, wi, WT, W0h, W256h, wg, wu, wd);
  k1_fused<<<224 + 256, 256, 0, stream>>>(xn, wg, wu, h, XT, YT, WT, W0h, W256h);
  k2_gemm2<<<256, 256, 0, stream>>>(h, wd, g);
  k3_final<<<256, 512, 0, stream>>>(YT, x, g, out);
}
```
